```python
import torch

def _softmax(x, axis=-1):
    return torch.softmax(x, dim=axis)
def _gelu(x):
    return torch.nn.functional.gelu(x, approximate="tanh")
def _silu(x):
    return torch.nn.functional.silu(x)
def _sigmoid(x):
    return torch.sigmoid(x)
def _layernorm(x, axis=-1, eps=1e-5):
    m = x.mean(dim=axis, keepdim=True)
    v = x.var(dim=axis, keepdim=True, unbiased=False)
    return (x - m) / torch.sqrt(v + eps)
def _scatter_add(tgt, idx, src, axis=0):
    return tgt.clone().index_add_(axis, idx, src)
def _segment_sum(src, seg, n):
    out = torch.zeros((n,) + src.shape[1:], dtype=src.dtype)
    return out.index_add_(0, seg, src)
def _pad(x, pw):
    flat = tuple(v for pair in reversed(pw) for v in pair)
    return torch.nn.functional.pad(x, flat)

kernel_name = 'gated_mix__mha_block_cached__glu_gated_swiglu__decode'

def setup_inputs():

    _w = (lambda a: (lambda t: t.to(torch.bfloat16) if t.is_floating_point() else t)(torch.as_tensor(a)))
    def _A_setup():
        B, Sq, D = 1, 1, 640
        H, Sk, Dh = 32, 8192, 192
        return {
            "x": _w((lambda *s: torch.randn(*s, dtype=torch.bfloat16) * 0.05)(B, Sq, D)),
            "k_cache": _w((lambda *s: torch.randn(*s, dtype=torch.bfloat16) * 3.2)(B, H, Sk, Dh)),
            "v_cache": _w((lambda *s: torch.randn(*s, dtype=torch.bfloat16) * 0.2)(B, H, Sk, Dh)),
            "wq": _w((lambda *s: torch.randn(*s, dtype=torch.bfloat16) * 3.2)(D, H * Dh) * (D ** -0.5)),
            "wo": _w((lambda *s: torch.randn(*s, dtype=torch.bfloat16) * 0.2)(H * Dh, D) * ((H * Dh) ** -0.5)),
        }
    _A_in = _A_setup()

    def _B_setup():
        B, S, D, F = 1, 1, 640, 1536
        return {
            "x": _w((lambda *s: torch.randn(*s, dtype=torch.bfloat16) * 0.05)(B, S, D)),
            "w1": _w((lambda *s: torch.randn(*s, dtype=torch.bfloat16) * 0.4)(D, F) * (2.0 / D) ** 0.5),
            "w3": _w((lambda *s: torch.randn(*s, dtype=torch.bfloat16) * 0.4)(D, F) * (2.0 / D) ** 0.5),
            "w2": _w((lambda *s: torch.randn(*s, dtype=torch.bfloat16) * 0.4)(F, D) * (2.0 / F) ** 0.5),
        }
    _B_in = _B_setup()

    return {
        "A_x": _A_in["x"],
        "A_k_cache": _A_in["k_cache"],
        "A_v_cache": _A_in["v_cache"],
        "A_wq": _A_in["wq"],
        "A_wo": _A_in["wo"],
        "B_x": _B_in["x"],
        "B_w1": _B_in["w1"],
        "B_w3": _B_in["w3"],
        "B_w2": _B_in["w2"],
        "gate_w": _w((lambda *s: torch.randn(*s, dtype=torch.bfloat16) * 0.05)(640) * 16.0),
    }

def reference(A_x, A_k_cache, A_v_cache, A_wq, A_wo, B_x, B_w1, B_w3, B_w2, gate_w):
    def _A_ref(x, k_cache, v_cache, wq, wo):
        B, Sq, D = x.shape
        H, Dh = 32, 192
        q = (lambda x, *d: x.permute(*d))((x @ wq).reshape(B, Sq, H, Dh), 0, 2, 1, 3)
        scores = torch.einsum("bhqd,bhkd->bhqk", q, k_cache) * (Dh ** -0.5)
        p = _softmax(scores, axis=-1)
        o = torch.einsum("bhqk,bhkd->bhqd", p, v_cache)
        return (lambda x, *d: x.permute(*d))(o, 0, 2, 1, 3).reshape(B, Sq, H * Dh) @ wo

    def _B_ref(x, w1, w3, w2):
        gate = torch.einsum("bsd,df->bsf", x, w1)
        up = torch.einsum("bsd,df->bsf", x, w3)
        h = _silu(gate) * up
        out = torch.einsum("bsf,fd->bsd", h, w2)
        return out

    x = A_x
    g = _sigmoid((x * gate_w).sum(-1, keepdims=True))
    a = _A_ref(x, A_k_cache, A_v_cache, A_wq, A_wo)
    b = _B_ref(x, B_w1, B_w3, B_w2)
    return g * a + (1 - g) * b
```

```cpp
#include <hip/hip_runtime.h>
#include <hip/hip_bf16.h>
#include <stdint.h>

typedef unsigned u32x4 __attribute__((ext_vector_type(4)));
typedef __bf16 bf16x2 __attribute__((ext_vector_type(2)));

constexpr int D = 640, H = 32, SK = 8192, DH = 192, F = 1536;
constexpr int NSPLIT = 8, KPS = SK / NSPLIT;

__device__ __forceinline__ float bf_lo(unsigned u) { return __builtin_bit_cast(float, u << 16); }
__device__ __forceinline__ float bf_hi(unsigned u) { return __builtin_bit_cast(float, u & 0xffff0000u); }
__device__ __forceinline__ float bf_round(float x) { return (float)(__bf16)x; }
__device__ __forceinline__ float dot2(unsigned k, unsigned q, float acc) {
  return __builtin_amdgcn_fdot2_f32_bf16(__builtin_bit_cast(bf16x2, k), __builtin_bit_cast(bf16x2, q), acc, false);
}
template <int CTRL> __device__ __forceinline__ float dpp(float x) {
  return __builtin_bit_cast(float, __builtin_amdgcn_mov_dpp(__builtin_bit_cast(int, x), CTRL, 0xf, 0xf, true));
}
constexpr int XOR1 = 0xB1, XOR2 = 0x4E, XOR7 = 0x141, XOR8 = 0x128;

constexpr size_t WS_PROJ = 0;
constexpr size_t WS_PART = 16384;
constexpr size_t WS_Y = WS_PART + 256 * 256;

__global__ void __launch_bounds__(256) k_proj(const uint16_t* __restrict__ x, const uint16_t* __restrict__ wq,
                                              const uint16_t* __restrict__ w1, const uint16_t* __restrict__ w3,
                                              float* __restrict__ ws) {
  __shared__ float red[32][65];
  __shared__ float xs[D];
  const int tid = threadIdx.x, c = tid & 7, rg = tid >> 3;
  for (int i = tid; i < D; i += 256) xs[i] = __builtin_bit_cast(float, (unsigned)x[i] << 16);
  __syncthreads();
  int col0 = blockIdx.x * 64;
  const uint16_t* W; int ld, cc;
  if (col0 < 6144) { W = wq; ld = 6144; cc = col0; }
  else if (col0 < 6144 + 1536) { W = w1; ld = 1536; cc = col0 - 6144; }
  else { W = w3; ld = 1536; cc = col0 - 6144 - 1536; }
  float acc[8] = {0, 0, 0, 0, 0, 0, 0, 0};
  u32x4 w[20];
#pragma unroll
  for (int i = 0; i < 20; ++i) w[i] = *(const u32x4*)(W + (size_t)(rg + 32 * i) * ld + cc + c * 8);
#pragma unroll
  for (int i = 0; i < 20; ++i) {
    float xv = xs[rg + 32 * i];
#pragma unroll
    for (int k = 0; k < 4; ++k) { acc[2 * k] += xv * bf_lo(w[i][k]); acc[2 * k + 1] += xv * bf_hi(w[i][k]); }
  }
#pragma unroll
  for (int k = 0; k < 8; ++k) red[rg][c * 8 + k] = acc[k];
  __syncthreads();
  if (tid < 64) {
    float s = 0;
    for (int r = 0; r < 32; ++r) s += red[r][tid];
    ws[WS_PROJ + col0 + tid] = bf_round(s);
  }
}

constexpr int AW = 8;
__global__ void __launch_bounds__(512) k_attn(const uint16_t* __restrict__ kc, const uint16_t* __restrict__ vc,
                                              float* __restrict__ ws) {
  __shared__ float mrg[AW][200];
  const int b = blockIdx.x, h = b >> 3, s = b & 7;
  const int tid = threadIdx.x, lane = tid & 63, wave = __builtin_amdgcn_readfirstlane(tid >> 6);
  const int a = lane >> 3, c = lane & 7;
  unsigned qp[3][4];
  {
    const float* q = ws + WS_PROJ + h * DH;
#pragma unroll
    for (int j = 0; j < 3; ++j)
#pragma unroll
      for (int k = 0; k < 4; ++k) {
        float lo = q[j * 64 + c * 8 + 2 * k], hi = q[j * 64 + c * 8 + 2 * k + 1];
        qp[j][k] = (__builtin_bit_cast(unsigned, lo) >> 16) | (__builtin_bit_cast(unsigned, hi) & 0xffff0000u);
      }
  }
  constexpr int STEPS = KPS / AW / 8;
  const size_t base = ((size_t)h * SK + (size_t)s * KPS + wave * (KPS / AW) + a) * DH + c * 8;
  const uint16_t* kp = kc + base;
  const uint16_t* vp = vc + base;
  constexpr float C2 = 0.07216878364870322f * 1.4426950408889634f;
  float m = -INFINITY, l = 0.f, acc[24];
#pragma unroll
  for (int i = 0; i < 24; ++i) acc[i] = 0.f;
  constexpr int DEPTH = 4;
  u32x4 K[DEPTH][3], V[DEPTH][3];
#pragma unroll
  for (int i = 0; i < DEPTH; ++i) {
#pragma unroll
    for (int j = 0; j < 3; ++j) K[i][j] = __builtin_nontemporal_load((const u32x4*)(kp + (size_t)i * 8 * DH + j * 64));
#pragma unroll
    for (int j = 0; j < 3; ++j) V[i][j] = __builtin_nontemporal_load((const u32x4*)(vp + (size_t)i * 8 * DH + j * 64));
  }
#pragma unroll
  for (int it = 0; it < STEPS; ++it) {
    const int sl = it % DEPTH;
    float d = 0.f;
#pragma unroll
    for (int j = 0; j < 3; ++j)
#pragma unroll
      for (int k = 0; k < 4; ++k) d = dot2(K[sl][j][k], qp[j][k], d);
    d += dpp<XOR1>(d); d += dpp<XOR2>(d); d += dpp<XOR7>(d);
    const float sc = d * C2;
    const float mn = fmaxf(m, sc);
    const float alpha = __builtin_amdgcn_exp2f(m - mn), p = __builtin_amdgcn_exp2f(sc - mn);
    l = l * alpha + p; m = mn;
#pragma unroll
    for (int j = 0; j < 3; ++j)
#pragma unroll
      for (int k = 0; k < 4; ++k) {
        acc[j * 8 + 2 * k] = acc[j * 8 + 2 * k] * alpha + p * bf_lo(V[sl][j][k]);
        acc[j * 8 + 2 * k + 1] = acc[j * 8 + 2 * k + 1] * alpha + p * bf_hi(V[sl][j][k]);
      }
    if (it + DEPTH < STEPS) {
#pragma unroll
      for (int j = 0; j < 3; ++j) K[sl][j] = __builtin_nontemporal_load((const u32x4*)(kp + (size_t)(it + DEPTH) * 8 * DH + j * 64));
#pragma unroll
      for (int j = 0; j < 3; ++j) V[sl][j] = __builtin_nontemporal_load((const u32x4*)(vp + (size_t)(it + DEPTH) * 8 * DH + j * 64));
    }
  }
  float M = m;
  M = fmaxf(M, __shfl_xor(M, 8)); M = fmaxf(M, __shfl_xor(M, 16)); M = fmaxf(M, __shfl_xor(M, 32));
  const float w = __builtin_amdgcn_exp2f(m - M);
  l *= w;
  l += __shfl_xor(l, 8); l += __shfl_xor(l, 16); l += __shfl_xor(l, 32);
#pragma unroll
  for (int i = 0; i < 24; ++i) { float t = acc[i] * w; t += __shfl_xor(t, 8); t += __shfl_xor(t, 16); t += __shfl_xor(t, 32); acc[i] = t; }
  if (a == 0) {
#pragma unroll
    for (int j = 0; j < 3; ++j)
#pragma unroll
      for (int k = 0; k < 8; ++k) mrg[wave][j * 64 + c * 8 + k] = acc[j * 8 + k];
    if (c == 0) { mrg[wave][192] = M; mrg[wave][193] = l; }
  }
  __syncthreads();
  if (tid < 194) {
    float MM = -INFINITY;
#pragma unroll
    for (int w2 = 0; w2 < AW; ++w2) MM = fmaxf(MM, mrg[w2][192]);
    float o = 0.f;
#pragma unroll
    for (int w2 = 0; w2 < AW; ++w2) o += __builtin_amdgcn_exp2f(mrg[w2][192] - MM) * mrg[w2][tid == 192 ? 193 : tid];
    float* out = ws + WS_PART + (size_t)b * 256;
    if (tid < 192) out[tid] = o;
    else if (tid == 192) { out[192] = MM; out[193] = o; }
  }
}

__global__ void __launch_bounds__(256) k_out(const uint16_t* __restrict__ wo, const uint16_t* __restrict__ w2, float* __restrict__ ws) {
  __shared__ float o[192];
  __shared__ float red[24][81];
  const int rb = blockIdx.x >> 3, s = blockIdx.x & 7, tid = threadIdx.x;
  if (tid < 192) {
    if (rb < 32) {
      const float* P = ws + WS_PART + (size_t)rb * 8 * 256;
      float M = -INFINITY;
      for (int i = 0; i < 8; ++i) M = fmaxf(M, P[i * 256 + 192]);
      float L = 0.f, acc = 0.f;
      for (int i = 0; i < 8; ++i) { float w = __builtin_amdgcn_exp2f(P[i * 256 + 192] - M); L += w * P[i * 256 + 193]; acc += w * P[i * 256 + tid]; }
      o[tid] = bf_round(acc / L);
    } else {
      int f = (rb - 32) * 192 + tid;
      float g = ws[WS_PROJ + 6144 + f], u = ws[WS_PROJ + 6144 + 1536 + f];
      float sl = bf_round(g / (1.f + __expf(-g)));
      o[tid] = bf_round(sl * u);
    }
  }
  __syncthreads();
  const uint16_t* W = rb < 32 ? wo + (size_t)rb * 192 * D : w2 + (size_t)(rb - 32) * 192 * D;
  if (tid < 240) {
    const int rg = tid / 10, c = tid % 10;
    float acc[8] = {0, 0, 0, 0, 0, 0, 0, 0};
#pragma unroll
    for (int i = 0; i < 8; ++i) {
      const int r = rg + 24 * i;
      u32x4 w = *(const u32x4*)(W + (size_t)r * D + s * 80 + c * 8);
      float ov = o[r];
#pragma unroll
      for (int k = 0; k < 4; ++k) { acc[2 * k] += ov * bf_lo(w[k]); acc[2 * k + 1] += ov * bf_hi(w[k]); }
    }
#pragma unroll
    for (int k = 0; k < 8; ++k) red[rg][c * 8 + k] = acc[k];
  }
  __syncthreads();
  if (tid < 80) {
    float t = 0;
    for (int r = 0; r < 24; ++r) t += red[r][tid];
    ws[WS_Y + rb * D + s * 80 + tid] = t;
  }
}

__global__ void __launch_bounds__(640) k_final(const uint16_t* __restrict__ x, const uint16_t* __restrict__ gw, const float* __restrict__ ws, __hip_bfloat16* __restrict__ out) {
  __shared__ float red[10];
  const int tid = threadIdx.x;
  float t = bf_round(__builtin_bit_cast(float, (unsigned)x[tid] << 16) * __builtin_bit_cast(float, (unsigned)gw[tid] << 16));
  for (int o = 32; o > 0; o >>= 1) t += __shfl_xor(t, o);
  if ((tid & 63) == 0) red[tid >> 6] = t;
  __syncthreads();
  float z = 0;
  for (int i = 0; i < 10; ++i) z += red[i];
  z = bf_round(z);
  const float g = bf_round(1.f / (1.f + __expf(-z)));
  float a = 0, b = 0;
  for (int h = 0; h < 32; ++h) a += ws[WS_Y + h * D + tid];
  for (int h = 32; h < 40; ++h) b += ws[WS_Y + h * D + tid];
  a = bf_round(a); b = bf_round(b);
  float r = bf_round(g * a) + bf_round(bf_round(1.f - g) * b);
  ((__bf16*)out)[tid] = (__bf16)r;
}

extern "C" void kernel_launch(void* const* d_in, const int* in_sizes, int n_in, void* d_out, int out_size, void* d_ws, size_t ws_size,
                              hipStream_t stream) {
  const uint16_t* x = (const uint16_t*)d_in[0];
  const uint16_t* kc = (const uint16_t*)d_in[1];
  const uint16_t* vc = (const uint16_t*)d_in[2];
  const uint16_t* wq = (const uint16_t*)d_in[3];
  const uint16_t* wo = (const uint16_t*)d_in[4];
  const uint16_t* w1 = (const uint16_t*)d_in[6];
  const uint16_t* w3 = (const uint16_t*)d_in[7];
  const uint16_t* w2 = (const uint16_t*)d_in[8];
  const uint16_t* gw = (const uint16_t*)d_in[9];
  float* ws = (float*)d_ws;
  k_proj<<<144, 256, 0, stream>>>(x, wq, w1, w3, ws);
  k_attn<<<256, 512, 0, stream>>>(kc, vc, ws);
  k_out<<<320, 256, 0, stream>>>(wo, w2, ws);
  k_final<<<1, 640, 0, stream>>>(x, gw, ws, (__hip_bfloat16*)d_out);
}
```

```cpp
#include <hip/hip_runtime.h>
#include <hip/hip_bf16.h>
#include <stdint.h>

typedef unsigned u32x4 __attribute__((ext_vector_type(4)));
typedef unsigned u32x2 __attribute__((ext_vector_type(2)));
typedef float f32x4 __attribute__((ext_vector_type(4)));
typedef __bf16 bf16x2 __attribute__((ext_vector_type(2)));
typedef __amdgpu_buffer_rsrc_t rsrc_t;

constexpr int D = 640, H = 32, SK = 8192, DH = 192, F = 1536;
constexpr int NHELP = 5, NSTR = 4, NWAVES = NHELP + NSTR, NTHR = NWAVES * 64;
constexpr int AUX_NT = 2, AUX_SC1 = 16;
constexpr unsigned WS_QO = 0;
constexpr unsigned WS_FO = WS_QO + 256 * 192 * 4;
constexpr unsigned WS_P1 = WS_FO + 2 * 8 * 1536 * 4;
constexpr unsigned WS_P2 = WS_P1 + 256 * 1024;
constexpr unsigned WS_END = WS_P2 + 256 * 320 * 4;
constexpr unsigned WS_STAMP = 1u << 20;
constexpr unsigned SENT = 0xFFFFFFFFu;
#define INF 2
#define RING 4

__device__ __forceinline__ float bf_lo(unsigned u) { return __builtin_bit_cast(float, u << 16); }
__device__ __forceinline__ float bf_hi(unsigned u) { return __builtin_bit_cast(float, u & 0xffff0000u); }
__device__ __forceinline__ float dot2(unsigned k, unsigned q, float acc) {
  return __builtin_amdgcn_fdot2_f32_bf16(__builtin_bit_cast(bf16x2, k), __builtin_bit_cast(bf16x2, q), acc, false);
}
template <int CTRL> __device__ __forceinline__ float dpp(float x) {
  return __builtin_bit_cast(float, __builtin_amdgcn_mov_dpp(__builtin_bit_cast(int, x), CTRL, 0xf, 0xf, true));
}
constexpr int XOR1 = 0xB1, XOR2 = 0x4E, HMIR = 0x141, XOR8 = 0x128;
__device__ __forceinline__ float sum16(float x) {
  auto s = __builtin_amdgcn_permlane16_swap(__float_as_uint(x), __float_as_uint(x), false, false);
  return __uint_as_float(s[0]) + __uint_as_float(s[1]);
}
__device__ __forceinline__ float sum32(float x) {
  auto s = __builtin_amdgcn_permlane32_swap(__float_as_uint(x), __float_as_uint(x), false, false);
  return __uint_as_float(s[0]) + __uint_as_float(s[1]);
}
__device__ __forceinline__ float max16(float x) {
  auto s = __builtin_amdgcn_permlane16_swap(__float_as_uint(x), __float_as_uint(x), false, false);
  return fmaxf(__uint_as_float(s[0]), __uint_as_float(s[1]));
}
__device__ __forceinline__ float max32(float x) {
  auto s = __builtin_amdgcn_permlane32_swap(__float_as_uint(x), __float_as_uint(x), false, false);
  return fmaxf(__uint_as_float(s[0]), __uint_as_float(s[1]));
}
__device__ __forceinline__ float sum_hi3(float x) { x += dpp<XOR8>(x); x = sum16(x); return sum32(x); }
__device__ __forceinline__ float sum_lo3(float x) { x += dpp<XOR1>(x); x += dpp<XOR2>(x); x += dpp<HMIR>(x); return x; }
__device__ __forceinline__ unsigned san(float v) { unsigned u = __float_as_uint(v); return u < 0xFFFFFFFEu ? u : 0xFFFFFFFEu; }
__device__ __forceinline__ rsrc_t mkrsrc(const void* p, unsigned bytes) { return __builtin_amdgcn_make_buffer_rsrc((void*)p, 0, bytes, 0x00020000); }
__device__ __forceinline__ u32x4 ld_nt(rsrc_t r, unsigned voff, unsigned soff) { return __builtin_amdgcn_raw_buffer_load_b128(r, voff, soff, AUX_NT); }
__device__ __forceinline__ u32x4 ld_sc1(rsrc_t r, unsigned voff) { return __builtin_amdgcn_raw_buffer_load_b128(r, voff, 0, AUX_SC1); }
__device__ __forceinline__ void st_sc1(rsrc_t r, unsigned voff, u32x4 v) { __builtin_amdgcn_raw_buffer_store_b128(v, r, voff, 0, AUX_SC1); }
__device__ __forceinline__ bool valid4(u32x4 v) { return v.x != SENT && v.y != SENT && v.z != SENT && v.w != SENT; }
__device__ __forceinline__ unsigned long long rt() { return __builtin_amdgcn_s_memrealtime(); }
constexpr unsigned SPIN_CAP = 1u << 18;

#define STAMP(i) do {} while (0)

struct Lds {
  unsigned qs[96];
  float mrg[NSTR][200];
  float hs[NHELP][16];
  unsigned qflag[4];
  unsigned scount;
  unsigned sdone;
};

struct GemvLoads { u32x4 w[10]; u32x4 xa; unsigned xb; };
__device__ __forceinline__ void gemv_issue(GemvLoads& g, rsrc_t xr, rsrc_t wr, unsigned ldB, unsigned row0, unsigned colB, int lane) {
  const unsigned rg = lane >> 3, c = lane & 7;
  const unsigned r0 = row0 + rg * 10;
  g.xa = __builtin_amdgcn_raw_buffer_load_b128(xr, r0 * 2, 0, 0);
  g.xb = __builtin_amdgcn_raw_buffer_load_b32(xr, r0 * 2 + 16, 0, 0);
  const unsigned off = r0 * ldB + colB + c * 16;
#pragma unroll
  for (int i = 0; i < 10; ++i) g.w[i] = ld_nt(wr, off + i * ldB, 0);
}
__device__ __forceinline__ void gemv_compute(const GemvLoads& g, float (&acc)[8]) {
#pragma unroll
  for (int k = 0; k < 8; ++k) acc[k] = 0.f;
#pragma unroll
  for (int i = 0; i < 10; ++i) {
    const unsigned xw = i < 8 ? g.xa[i >> 1] : g.xb;
    const float xv = (i & 1) ? bf_hi(xw) : bf_lo(xw);
#pragma unroll
    for (int k = 0; k < 4; ++k) { acc[2 * k] = fmaf(xv, bf_lo(g.w[i][k]), acc[2 * k]); acc[2 * k + 1] = fmaf(xv, bf_hi(g.w[i][k]), acc[2 * k + 1]); }
  }
#pragma unroll
  for (int k = 0; k < 8; ++k) acc[k] = sum_hi3(acc[k]);
}

__global__ void __launch_bounds__(NTHR) fused_kernel(const uint16_t* __restrict__ x, const uint16_t* __restrict__ kc, const uint16_t* __restrict__ vc,
                                                     const uint16_t* __restrict__ wq, const uint16_t* __restrict__ wo, const uint16_t* __restrict__ w1,
                                                     const uint16_t* __restrict__ w3, const uint16_t* __restrict__ w2, const uint16_t* __restrict__ gw,
                                                     unsigned char* __restrict__ ws, uint16_t* __restrict__ out) {
  __shared__ __attribute__((aligned(16))) Lds L;
  const int b = blockIdx.x, h = b >> 3, s = b & 7;
  const int tid = threadIdx.x, lane = tid & 63, wave = __builtin_amdgcn_readfirstlane(tid >> 6);
  const int rg = lane >> 3, c = lane & 7;
  STAMP(0);
  const rsrc_t wsr = mkrsrc(ws, WS_END);
  if (wave < NHELP) {
    const int j = wave;
    const int rq = s >> 1, half = s & 1;
    const rsrc_t xr = mkrsrc(x, D * 2);
    if (tid < 8) { if (tid < 4) L.qflag[tid] = 0; else if (tid == 4) L.scount = 0; else if (tid == 5) L.sdone = 0; }
    GemvLoads g;
    int unit = -1;
    if (j < 3) {
      gemv_issue(g, xr, mkrsrc(wq, D * H * DH * 2), H * DH * 2, s * 80, (h * DH + j * 64) * 2, lane);
    } else {
      unit = j == 3 ? b : ((b & 1) ? -1 : 256 + (b >> 1));
      const int u = unit < 0 ? 0 : unit;
      const int mat = u / 192, lb = (u % 192) >> 3, rp = u & 7;
      gemv_issue(g, xr, mkrsrc(mat ? w3 : w1, unit < 0 ? 0 : D * F * 2), F * 2, rp * 80, lb * 128, lane);
    }
    __builtin_amdgcn_sched_barrier(0);
    asm volatile("s_waitcnt lgkmcnt(0)" ::: "memory");
    __builtin_amdgcn_s_barrier();
    __builtin_amdgcn_sched_barrier(0);
    const u32x4 gxa = __builtin_amdgcn_raw_buffer_load_b128(xr, lane * 20, 0, 0);
    const unsigned gxb = __builtin_amdgcn_raw_buffer_load_b32(xr, lane * 20 + 16, 0, 0);
    const rsrc_t gr = mkrsrc(gw, D * 2);
    const u32x4 gwa = __builtin_amdgcn_raw_buffer_load_b128(gr, lane * 20, 0, 0);
    const unsigned gwb = __builtin_amdgcn_raw_buffer_load_b32(gr, lane * 20 + 16, 0, 0);
    u32x4 wot[6], w2t[2];
    const unsigned colB = (half * 320 + j * 64 + c * 8) * 2;
    {
      const rsrc_t wor = mkrsrc(wo, H * DH * D * 2);
      const unsigned off = (unsigned)(h * DH + rq * 48 + rg * 6) * (D * 2) + colB;
#pragma unroll
      for (int i = 0; i < 6; ++i) wot[i] = ld_nt(wor, off + i * (D * 2), 0);
      const rsrc_t w2r = mkrsrc(w2, F * D * 2);
      const unsigned f0 = (h * 4 + rq) * 12;
      w2t[0] = ld_nt(w2r, (f0 + rg) * (D * 2) + colB, 0);
      w2t[1] = ld_nt(w2r, rg < 4 ? (f0 + 8 + rg) * (D * 2) + colB : 0x7fffffffu, 0);
    }
    __builtin_amdgcn_sched_barrier(0);
    float pa[8];
    gemv_compute(g, pa);
    STAMP(1);
    {
      const u32x4 v = rg == 0 ? u32x4{san(pa[0]), san(pa[1]), san(pa[2]), san(pa[3])} : u32x4{san(pa[4]), san(pa[5]), san(pa[6]), san(pa[7])};
      unsigned off;
      if (j < 3) off = WS_QO + (b * 192 + j * 64 + c * 8 + rg * 4) * 4;
      else { const int u = unit < 0 ? 0 : unit; const int mat = u / 192, lb = (u % 192) >> 3, rp = u & 7; off = WS_FO + ((mat * 8 + rp) * 1536 + lb * 64 + c * 8 + rg * 4) * 4; }
      if (rg >= 2 || (j >= 3 && unit < 0)) off = 0x7fffffffu;
      st_sc1(wsr, off, v);
    }
    if (j < 3) {
      const int p = lane >> 4, c4 = lane & 15;
      const unsigned o0 = WS_QO + ((h * 8 + p) * 192 + j * 64 + c4 * 4) * 4, o1 = o0 + 4 * 192 * 4;
      u32x4 a0, a1;
      for (unsigned spin = 0; spin < SPIN_CAP; ++spin) {
        asm volatile("" ::: "memory");
        a0 = ld_sc1(wsr, o0); a1 = ld_sc1(wsr, o1);
        if (__all(valid4(a0) && valid4(a1))) break;
        __builtin_amdgcn_s_sleep(1);
      }
      float q[4];
#pragma unroll
      for (int k = 0; k < 4; ++k) { float t = __uint_as_float(a0[k]) + __uint_as_float(a1[k]); t = sum16(t); q[k] = sum32(t); }
      if (p == 0) {
        bf16x2 lo = {(__bf16)q[0], (__bf16)q[1]}, hi = {(__bf16)q[2], (__bf16)q[3]};
        *(u32x2*)&L.qs[j * 32 + c4 * 2] = u32x2{__builtin_bit_cast(unsigned, lo), __builtin_bit_cast(unsigned, hi)};
      }
      asm volatile("s_waitcnt lgkmcnt(0)" ::: "memory");
      if (lane == 0) __hip_atomic_store(&L.qflag[j], 1u, __ATOMIC_RELAXED, __HIP_MEMORY_SCOPE_WORKGROUP);
      STAMP(2);
    }
    float gate;
    {
      float z = 0.f;
#pragma unroll
      for (int k = 0; k < 4; ++k) { z = fmaf(bf_lo(gxa[k]), bf_lo(gwa[k]), z); z = fmaf(bf_hi(gxa[k]), bf_hi(gwa[k]), z); }
      z = fmaf(bf_lo(gxb), bf_lo(gwb), z); z = fmaf(bf_hi(gxb), bf_hi(gwb), z);
      z = sum_hi3(sum_lo3(z));
      gate = 1.f / (1.f + __expf(-z));
    }
    float y[8];
    {
      const unsigned f0 = (h * 4 + rq) * 12;
      const int part = lane >> 4, mr = lane & 15;
      const unsigned o = part < 3 ? WS_FO + (mr * 1536 + f0 + part * 4) * 4 : 0x7fffffffu;
      u32x4 a;
      for (unsigned spin = 0; spin < SPIN_CAP; ++spin) {
        asm volatile("" ::: "memory");
        a = ld_sc1(wsr, o);
        if (__all(valid4(a))) break;
        __builtin_amdgcn_s_sleep(8);
      }
      float hv[4];
#pragma unroll
      for (int k = 0; k < 4; ++k) {
        const float t = sum_lo3(__uint_as_float(a[k]));
        const float o8 = dpp<XOR8>(t);
        const float gv = (mr & 8) ? o8 : t, uv = (mr & 8) ? t : o8;
        hv[k] = gv / (1.f + __expf(-gv)) * uv;
      }
      if (mr == 0 && part < 3) *(f32x4*)&L.hs[j][part * 4] = f32x4{hv[0], hv[1], hv[2], hv[3]};
      asm volatile("s_waitcnt lgkmcnt(0)" ::: "memory");
      const float h0 = L.hs[j][rg] * (1.f - gate), h1 = (rg < 4 ? L.hs[j][8 + rg] : 0.f) * (1.f - gate);
#pragma unroll
      for (int k = 0; k < 4; ++k) {
        y[2 * k] = h0 * bf_lo(w2t[0][k]) + h1 * bf_lo(w2t[1][k]);
        y[2 * k + 1] = h0 * bf_hi(w2t[0][k]) + h1 * bf_hi(w2t[1][k]);
      }
    }
    STAMP(3);
    for (unsigned spin = 0; spin < SPIN_CAP * 16; ++spin) {
      if (__hip_atomic_load(&L.sdone, __ATOMIC_RELAXED, __HIP_MEMORY_SCOPE_WORKGROUP)) break;
      __builtin_amdgcn_s_sleep(1);
    }
    STAMP(4);
    {
      const unsigned o = WS_P1 + (h * 8) * 1024 + (rq * 8 + rg) * 32;
      u32x4 A[8], B[8];
      for (unsigned spin = 0; spin < SPIN_CAP; ++spin) {
        asm volatile("" ::: "memory");
        bool ok = true;
#pragma unroll
        for (int p = 0; p < 8; ++p) { A[p] = ld_sc1(wsr, o + p * 1024); B[p] = ld_sc1(wsr, o + p * 1024 + 16); }
#pragma unroll
        for (int p = 0; p < 8; ++p) ok = ok && valid4(A[p]) && valid4(B[p]);
        if (__all(ok)) break;
        __builtin_amdgcn_s_sleep(1);
      }
      STAMP(5);
      float M = __uint_as_float(B[0].z);
#pragma unroll
      for (int p = 1; p < 8; ++p) M = fmaxf(M, __uint_as_float(B[p].z));
      float Ls = 0.f, o6[6] = {0, 0, 0, 0, 0, 0};
#pragma unroll
      for (int p = 0; p < 8; ++p) {
        const float w = __builtin_amdgcn_exp2f(__uint_as_float(B[p].z) - M);
        Ls = fmaf(w, __uint_as_float(B[p].w), Ls);
        o6[0] = fmaf(w, __uint_as_float(A[p].x), o6[0]); o6[1] = fmaf(w, __uint_as_float(A[p].y), o6[1]);
        o6[2] = fmaf(w, __uint_as_float(A[p].z), o6[2]); o6[3] = fmaf(w, __uint_as_float(A[p].w), o6[3]);
        o6[4] = fmaf(w, __uint_as_float(B[p].x), o6[4]); o6[5] = fmaf(w, __uint_as_float(B[p].y), o6[5]);
      }
      const float sc = gate / Ls;
#pragma unroll
      for (int i = 0; i < 6; ++i) {
        const float ov = o6[i] * sc;
#pragma unroll
        for (int k = 0; k < 4; ++k) { y[2 * k] = fmaf(ov, bf_lo(wot[i][k]), y[2 * k]); y[2 * k + 1] = fmaf(ov, bf_hi(wot[i][k]), y[2 * k + 1]); }
      }
#pragma unroll
      for (int k = 0; k < 8; ++k) y[k] = sum_hi3(y[k]);
      const u32x4 v = rg == 0 ? u32x4{san(y[0]), san(y[1]), san(y[2]), san(y[3])} : u32x4{san(y[4]), san(y[5]), san(y[6]), san(y[7])};
      const unsigned off = rg < 2 ? WS_P2 + ((half * 128 + h * 4 + rq) * 320 + j * 64 + c * 8 + rg * 4) * 4 : 0x7fffffffu;
      st_sc1(wsr, off, v);
    }
    STAMP(6);
    if (j == 0 && b < 160) {
      const int hf = b / 80, cc = (b % 80) * 4;
      const unsigned o0 = WS_P2 + ((hf * 128 + lane) * 320 + cc) * 4, o1 = o0 + 64 * 320 * 4;
      u32x4 a0, a1;
      for (unsigned spin = 0; spin < SPIN_CAP; ++spin) {
        asm volatile("" ::: "memory");
        a0 = ld_sc1(wsr, o0); a1 = ld_sc1(wsr, o1);
        if (__all(valid4(a0) && valid4(a1))) break;
        __builtin_amdgcn_s_sleep(1);
      }
      float r[4];
#pragma unroll
      for (int k = 0; k < 4; ++k) r[k] = sum_hi3(sum_lo3(__uint_as_float(a0[k]) + __uint_as_float(a1[k])));
      if (lane == 0) {
        bf16x2 lo = {(__bf16)r[0], (__bf16)r[1]}, hi = {(__bf16)r[2], (__bf16)r[3]};
        *(u32x2*)(out + b * 4) = u32x2{__builtin_bit_cast(unsigned, lo), __builtin_bit_cast(unsigned, hi)};
      }
      STAMP(7);
    }
  } else {
    const int sw = wave - NHELP;
    const rsrc_t kr = mkrsrc(kc + (size_t)h * SK * DH, SK * DH * 2), vr = mkrsrc(vc + (size_t)h * SK * DH, SK * DH * 2);
    const unsigned loff = (unsigned)(rg * DH + c * 8) * 2;
    constexpr int N = 32;
    constexpr unsigned STEPB = 8 * DH * 2;
    const unsigned wid = s * NSTR + sw;
    u32x4 K[RING][3], V[RING][3];
    auto issue = [&](int slot, unsigned unit) {
      const unsigned so = unit * STEPB;
#pragma unroll
      for (int jj = 0; jj < 3; ++jj) K[slot][jj] = ld_nt(kr, loff + jj * 128, so);
#pragma unroll
      for (int jj = 0; jj < 3; ++jj) V[slot][jj] = ld_nt(vr, loff + jj * 128, so);
    };
    __builtin_amdgcn_s_barrier();
    __builtin_amdgcn_sched_barrier(0);
#pragma unroll
    for (int i = 0; i < RING; ++i) {
      if (i >= INF) { if (INF == 2) asm volatile("s_waitcnt vmcnt(6)" ::: "memory"); else if (INF == 3) asm volatile("s_waitcnt vmcnt(12)" ::: "memory"); else asm volatile("s_waitcnt vmcnt(18)" ::: "memory"); }
      issue(i, i * 32 + wid);
      __builtin_amdgcn_sched_barrier(0);
    }
    STAMP(1);
    for (unsigned spin = 0; spin < SPIN_CAP * 16; ++spin) {
      const unsigned f0 = __hip_atomic_load(&L.qflag[0], __ATOMIC_RELAXED, __HIP_MEMORY_SCOPE_WORKGROUP);
      const unsigned f1 = __hip_atomic_load(&L.qflag[1], __ATOMIC_RELAXED, __HIP_MEMORY_SCOPE_WORKGROUP);
      const unsigned f2 = __hip_atomic_load(&L.qflag[2], __ATOMIC_RELAXED, __HIP_MEMORY_SCOPE_WORKGROUP);
      if (f0 & f1 & f2) break;
      __builtin_amdgcn_s_sleep(1);
    }
    asm volatile("" ::: "memory");
    u32x4 qp[3];
#pragma unroll
    for (int jj = 0; jj < 3; ++jj) qp[jj] = *(const u32x4*)&L.qs[jj * 32 + c * 4];
    STAMP(2);
    constexpr float C2 = 0.07216878364870322f * 1.4426950408889634f;
    float m = -INFINITY, l = 0.f, acc[24];
#pragma unroll
    for (int i = 0; i < 24; ++i) acc[i] = 0.f;
    auto consume = [&](int slot) {
      float d = 0.f;
#pragma unroll
      for (int jj = 0; jj < 3; ++jj)
#pragma unroll
        for (int k = 0; k < 4; ++k) d = dot2(K[slot][jj][k], qp[jj][k], d);
      d = sum_lo3(d);
      const float sc = d * C2;
      const float mn = fmaxf(m, sc);
      const float alpha = __builtin_amdgcn_exp2f(m - mn), p = __builtin_amdgcn_exp2f(sc - mn);
      l = fmaf(l, alpha, p); m = mn;
#pragma unroll
      for (int jj = 0; jj < 3; ++jj)
#pragma unroll
        for (int k = 0; k < 4; ++k) {
          acc[jj * 8 + 2 * k] = fmaf(p, bf_lo(V[slot][jj][k]), acc[jj * 8 + 2 * k] * alpha);
          acc[jj * 8 + 2 * k + 1] = fmaf(p, bf_hi(V[slot][jj][k]), acc[jj * 8 + 2 * k + 1] * alpha);
        }
    };
#pragma unroll
    for (int i = 0; i < RING; ++i) {
      consume(i);
      __builtin_amdgcn_sched_barrier(0);
      if (i < INF) issue(i, (RING + i) * 32 + wid);
      __builtin_amdgcn_sched_barrier(0);
    }
#pragma unroll
    for (int i = RING; i < N; ++i) {
      consume(i % RING);
      __builtin_amdgcn_sched_barrier(0);
      if (i + INF < N) issue((i + INF) % RING, (i + INF) * 32 + wid);
      __builtin_amdgcn_sched_barrier(0);
    }
    STAMP(3);
    float M = fmaxf(m, dpp<XOR8>(m)); M = max16(M); M = max32(M);
    const float w = __builtin_amdgcn_exp2f(m - M);
    l = sum_hi3(l * w);
#pragma unroll
    for (int i = 0; i < 24; ++i) acc[i] = sum_hi3(acc[i] * w);
    if (rg < 3) {
      const f32x4 v0 = rg == 0 ? f32x4{acc[0], acc[1], acc[2], acc[3]} : rg == 1 ? f32x4{acc[8], acc[9], acc[10], acc[11]} : f32x4{acc[16], acc[17], acc[18], acc[19]};
      const f32x4 v1 = rg == 0 ? f32x4{acc[4], acc[5], acc[6], acc[7]} : rg == 1 ? f32x4{acc[12], acc[13], acc[14], acc[15]} : f32x4{acc[20], acc[21], acc[22], acc[23]};
      *(f32x4*)&L.mrg[sw][rg * 64 + c * 8] = v0;
      *(f32x4*)&L.mrg[sw][rg * 64 + c * 8 + 4] = v1;
    } else if (lane == 24) { L.mrg[sw][192] = M; L.mrg[sw][193] = l; }
    asm volatile("s_waitcnt lgkmcnt(0)" ::: "memory");
    unsigned old = 0;
    if (lane == 0) old = __hip_atomic_fetch_add(&L.scount, 1u, __ATOMIC_RELAXED, __HIP_MEMORY_SCOPE_WORKGROUP);
    old = __builtin_amdgcn_readfirstlane(old);
    if (old == NSTR - 1) {
      asm volatile("" ::: "memory");
      const int slot = lane >> 1, hf = lane & 1;
      float MM = L.mrg[0][192];
#pragma unroll
      for (int q2 = 1; q2 < NSTR; ++q2) MM = fmaxf(MM, L.mrg[q2][192]);
      float o0 = 0.f, o1 = 0.f, o2 = 0.f, o3 = 0.f;
      const int i0 = slot * 6 + hf * 4;
#pragma unroll
      for (int q2 = 0; q2 < NSTR; ++q2) {
        const float ww = __builtin_amdgcn_exp2f(L.mrg[q2][192] - MM);
        o0 = fmaf(ww, L.mrg[q2][i0], o0); o1 = fmaf(ww, L.mrg[q2][i0 + 1], o1);
        o2 = fmaf(ww, L.mrg[q2][hf ? 192 : i0 + 2], o2); o3 = fmaf(ww, L.mrg[q2][hf ? 193 : i0 + 3], o3);
      }
      if (hf) o2 = MM;
      st_sc1(wsr, WS_P1 + b * 1024 + lane * 16, u32x4{san(o0), san(o1), san(o2), san(o3)});
      if (lane == 0) __hip_atomic_store(&L.sdone, 1u, __ATOMIC_RELAXED, __HIP_MEMORY_SCOPE_WORKGROUP);
      STAMP(4);
    }
  }
}

extern "C" void kernel_launch(void* const* d_in, const int* in_sizes, int n_in, void* d_out, int out_size, void* d_ws, size_t ws_size,
                              hipStream_t stream) {
  (void)hipMemsetAsync(d_ws, 0xFF, WS_END, stream);
  fused_kernel<<<256, NTHR, 0, stream>>>((const uint16_t*)d_in[0], (const uint16_t*)d_in[1], (const uint16_t*)d_in[2], (const uint16_t*)d_in[3],
                                         (const uint16_t*)d_in[4], (const uint16_t*)d_in[6], (const uint16_t*)d_in[7], (const uint16_t*)d_in[8],
                                         (const uint16_t*)d_in[9], (unsigned char*)d_ws, (uint16_t*)d_out);
}
```

```cpp
#include <hip/hip_runtime.h>
#include <hip/hip_bf16.h>
#include <stdint.h>

typedef unsigned u32x4 __attribute__((ext_vector_type(4)));
typedef unsigned u32x2 __attribute__((ext_vector_type(2)));
typedef float f32x4 __attribute__((ext_vector_type(4)));
typedef float f32x2 __attribute__((ext_vector_type(2)));
typedef __bf16 bf16x2 __attribute__((ext_vector_type(2)));
typedef __amdgpu_buffer_rsrc_t rsrc_t;

#define NSTR 4
#define RING 4
#define AHEAD 2
#define INF 2
#define DYN 1
#define NSTAT 24
#define TQ 4
#define DRY 1

constexpr int D = 640, H = 32, SK = 8192, DH = 192, F = 1536;
constexpr int NHELP = 5, NWAVES = NHELP + NSTR, NTHR = NWAVES * 64;
constexpr int AUX_NT = 2, AUX_SC1 = 16;
constexpr unsigned OOB = 0x7ffffff0u;
constexpr unsigned WS_QO = 0;
constexpr unsigned WS_FO = WS_QO + 256 * 192 * 4;
constexpr unsigned WS_P1 = WS_FO + 2 * 8 * 1536 * 4;
constexpr unsigned WS_P2 = WS_P1 + 256 * 1024;
constexpr unsigned WS_HS = WS_P2 + 160 * 2048;
constexpr unsigned WS_CNT = WS_HS + 256 * 512;
constexpr unsigned WS_END = WS_CNT + 32 * 256;
constexpr unsigned WS_STAMP = 2u << 20;
constexpr unsigned SENT = 0xFFFFFFFFu;

__device__ __forceinline__ float bf_lo(unsigned u) { return __builtin_bit_cast(float, u << 16); }
__device__ __forceinline__ float bf_hi(unsigned u) { return __builtin_bit_cast(float, u & 0xffff0000u); }
__device__ __forceinline__ float dot2(unsigned k, unsigned q, float acc) {
  return __builtin_amdgcn_fdot2_f32_bf16(__builtin_bit_cast(bf16x2, k), __builtin_bit_cast(bf16x2, q), acc, false);
}
template <int CTRL> __device__ __forceinline__ float dpp(float x) {
  return __builtin_bit_cast(float, __builtin_amdgcn_mov_dpp(__builtin_bit_cast(int, x), CTRL, 0xf, 0xf, true));
}
constexpr int XOR1 = 0xB1, XOR2 = 0x4E, HMIR = 0x141, XOR8 = 0x128;
__device__ __forceinline__ float tr16(float a, float b) {
  auto s = __builtin_amdgcn_permlane16_swap(__float_as_uint(a), __float_as_uint(b), false, false);
  return __uint_as_float(s[0]) + __uint_as_float(s[1]);
}
__device__ __forceinline__ float tr32(float a, float b) {
  auto s = __builtin_amdgcn_permlane32_swap(__float_as_uint(a), __float_as_uint(b), false, false);
  return __uint_as_float(s[0]) + __uint_as_float(s[1]);
}
__device__ __forceinline__ float max16(float x) {
  auto s = __builtin_amdgcn_permlane16_swap(__float_as_uint(x), __float_as_uint(x), false, false);
  return fmaxf(__uint_as_float(s[0]), __uint_as_float(s[1]));
}
__device__ __forceinline__ float max32(float x) {
  auto s = __builtin_amdgcn_permlane32_swap(__float_as_uint(x), __float_as_uint(x), false, false);
  return fmaxf(__uint_as_float(s[0]), __uint_as_float(s[1]));
}
__device__ __forceinline__ float sum_hi3(float x) { x += dpp<XOR8>(x); x = tr16(x, x); return tr32(x, x); }
__device__ __forceinline__ float sum_lo3(float x) { x += dpp<XOR1>(x); x += dpp<XOR2>(x); x += dpp<HMIR>(x); return x; }
__device__ __forceinline__ float max_lo3(float x) { x = fmaxf(x, dpp<XOR1>(x)); x = fmaxf(x, dpp<XOR2>(x)); return fmaxf(x, dpp<HMIR>(x)); }
__device__ __forceinline__ unsigned san(float v) { unsigned u = __float_as_uint(v); return u < 0xFFFFFFFEu ? u : 0xFFFFFFFEu; }
__device__ __forceinline__ rsrc_t mkrsrc(const void* p, unsigned bytes) { return __builtin_amdgcn_make_buffer_rsrc((void*)p, 0, bytes, 0x00020000); }
__device__ __forceinline__ u32x4 ld_nt(rsrc_t r, unsigned voff, unsigned soff) { return __builtin_amdgcn_raw_buffer_load_b128(r, voff, soff, AUX_NT); }
__device__ __forceinline__ u32x4 ld_sc1(rsrc_t r, unsigned voff) { return __builtin_amdgcn_raw_buffer_load_b128(r, voff, 0, AUX_SC1); }
__device__ __forceinline__ u32x2 ld8_sc1(rsrc_t r, unsigned voff) { return __builtin_amdgcn_raw_buffer_load_b64(r, voff, 0, AUX_SC1); }
__device__ __forceinline__ void st_sc1(rsrc_t r, unsigned voff, u32x4 v) { __builtin_amdgcn_raw_buffer_store_b128(v, r, voff, 0, AUX_SC1); }
__device__ __forceinline__ void st8_sc1(rsrc_t r, unsigned voff, u32x2 v) { __builtin_amdgcn_raw_buffer_store_b64(v, r, voff, 0, AUX_SC1); }
__device__ __forceinline__ bool valid4(u32x4 v) { return v.x != SENT && v.y != SENT && v.z != SENT && v.w != SENT; }
__device__ __forceinline__ unsigned lsb4(u32x4 v) { return (v.x & 1u) | ((v.y & 1u) << 1) | ((v.z & 1u) << 2) | ((v.w & 1u) << 3); }
__device__ __forceinline__ unsigned long long rt() { return __builtin_amdgcn_s_memrealtime(); }
__device__ __forceinline__ unsigned lds_ld(const unsigned* p) { return __hip_atomic_load(p, __ATOMIC_RELAXED, __HIP_MEMORY_SCOPE_WORKGROUP); }
__device__ __forceinline__ void lds_st(unsigned* p, unsigned v) { __hip_atomic_store(p, v, __ATOMIC_RELAXED, __HIP_MEMORY_SCOPE_WORKGROUP); }
constexpr unsigned GSPIN = 1u << 16;
constexpr unsigned LSPIN = 1u << 20;
template <int SLEEP> __device__ __forceinline__ void lds_wait_ge(const unsigned* p, unsigned v) {
#pragma nounroll
  for (unsigned spin = 0; spin < LSPIN; ++spin) {
    if (__builtin_amdgcn_readfirstlane(lds_ld(p)) >= v) break;
    __builtin_amdgcn_s_sleep(SLEEP);
  }
  asm volatile("" ::: "memory");
}
#define WAIT_VM0() asm volatile("s_waitcnt vmcnt(0)" ::: "memory")
#define WAIT_LKM0() asm volatile("s_waitcnt lgkmcnt(0)" ::: "memory")

#define STAMP(i) do {} while (0)
#define STAMP_FLUSH() do {} while (0)

template <int N> struct IC { static constexpr int value = N; };
template <int B, int E, class Fn> __device__ __forceinline__ void static_for(Fn&& f) { if constexpr (B < E) { f(IC<B>{}); static_for<B + 1, E>(f); } }

struct Lds {
  float mrg[NSTR + 1][256];
  float ml[NSTR + 1][2];
  unsigned qs[96];
  float os[64];
  float hs[16];
  unsigned qcnt;
  unsigned qpub;
  unsigned qrdy;
  unsigned bdone;
  unsigned hrdy;
  unsigned scount;
  unsigned oflag;
  unsigned dummy;
  unsigned tcons;
  unsigned tprod;
  unsigned pad[6];
  unsigned tq[320];
};

struct GemvLoads { u32x4 w[10]; u32x4 xa; unsigned xb; };
__device__ __forceinline__ void gemv_issue(GemvLoads& g, rsrc_t xr, rsrc_t wr, unsigned ldB, unsigned row0, unsigned colB, int lane) {
  const unsigned rg = lane >> 3, c = lane & 7;
  const unsigned r0 = row0 + rg * 10;
  const unsigned off = r0 * ldB + colB + c * 16;
#pragma unroll
  for (int i = 0; i < 10; ++i) g.w[i] = ld_nt(wr, off + i * ldB, 0);
  g.xa = __builtin_amdgcn_raw_buffer_load_b128(xr, r0 * 2, 0, 0);
  g.xb = __builtin_amdgcn_raw_buffer_load_b32(xr, r0 * 2 + 16, 0, 0);
}
__device__ __forceinline__ void gemv_compute(const GemvLoads& g, float (&r)[4]) {
  float acc[8];
#pragma unroll
  for (int k = 0; k < 8; ++k) acc[k] = 0.f;
#pragma unroll
  for (int i = 0; i < 10; ++i) {
    const unsigned xw = i < 8 ? g.xa[i >> 1] : g.xb;
    const float xv = (i & 1) ? bf_hi(xw) : bf_lo(xw);
#pragma unroll
    for (int k = 0; k < 4; ++k) { acc[2 * k] = fmaf(xv, bf_lo(g.w[i][k]), acc[2 * k]); acc[2 * k + 1] = fmaf(xv, bf_hi(g.w[i][k]), acc[2 * k + 1]); }
  }
#pragma unroll
  for (int k = 0; k < 4; ++k) { float t = tr32(acc[k], acc[k + 4]); t = tr16(t, t); r[k] = t + dpp<XOR8>(t); }
}

__global__ void __launch_bounds__(NTHR) fused_kernel(const uint16_t* __restrict__ x, const uint16_t* __restrict__ kc, const uint16_t* __restrict__ vc,
                                                     const uint16_t* __restrict__ wq, const uint16_t* __restrict__ wo, const uint16_t* __restrict__ w1,
                                                     const uint16_t* __restrict__ w3, const uint16_t* __restrict__ w2, const uint16_t* __restrict__ gw,
                                                     unsigned char* __restrict__ ws, uint16_t* __restrict__ out) {
  __shared__ __attribute__((aligned(16))) Lds L;
  const int b = blockIdx.x, h = b >> 3, s = b & 7;
  const int tid = threadIdx.x, lane = tid & 63, wave = __builtin_amdgcn_readfirstlane(tid >> 6);
  const int rg = lane >> 3, c = lane & 7;
  STAMP(0);
  asm volatile("" :: "s"(x), "s"(kc), "s"(vc), "s"(wq), "s"(wo), "s"(w1), "s"(w3), "s"(w2), "s"(gw), "s"(ws), "s"(out));
  const rsrc_t wsr = mkrsrc(ws, WS_END);
  int npass = DRY ? 2 : 1;
  asm volatile("" : "+s"(npass));
  const int j = wave;
  const int rq = s >> 1, half = s & 1;
  const bool wr16 = (lane & 0x18) == 0;
  const unsigned col4 = c * 8 + (lane >> 5) * 4;
  float gate = 0.f, y[8], m = -INFINITY, l = 0.f, acc[24];
  u32x4 wot[6];
#pragma unroll
  for (int i = 0; i < 24; ++i) acc[i] = 0.f;
#pragma unroll
  for (int i = 0; i < 8; ++i) y[i] = 0.f;
#pragma unroll
  for (int i = 0; i < 6; ++i) wot[i] = u32x4{0u, 0u, 0u, 0u};
  if (wave < NHELP) {
    const rsrc_t xr = mkrsrc(x, D * 2);
    if (j < 3) {
      const int p = lane >> 4, c4 = lane & 15;
      const unsigned o0 = WS_QO + ((h * 8 + p) * 192 + j * 64 + c4 * 4) * 4, o1 = o0 + 4 * 192 * 4;
      const unsigned om = wr16 ? WS_QO + (b * 192 + j * 64 + col4) * 4 : OOB;
      GemvLoads g;
      const u32x4 sn0 = ld_sc1(wsr, o0), sn1 = ld_sc1(wsr, o1), snm = ld_sc1(wsr, om);
      gemv_issue(g, xr, mkrsrc(wq, D * H * DH * 2), H * DH * 2, s * 80, (h * DH + j * 64) * 2, lane);
      __builtin_amdgcn_sched_barrier(0);
      if (tid < 10) (&L.qcnt)[tid] = 0;
      WAIT_LKM0();
      __builtin_amdgcn_s_barrier();
      __builtin_amdgcn_sched_barrier(0);
      const unsigned t0 = lsb4(sn0), t1 = lsb4(sn1);
      float pa[4];
      gemv_compute(g, pa);
      STAMP(1);
      st_sc1(wsr, om, u32x4{(__float_as_uint(pa[0]) & ~1u) | (~snm.x & 1u), (__float_as_uint(pa[1]) & ~1u) | (~snm.y & 1u),
                            (__float_as_uint(pa[2]) & ~1u) | (~snm.z & 1u), (__float_as_uint(pa[3]) & ~1u) | (~snm.w & 1u)});
      u32x4 a0, a1;
#pragma nounroll
      for (unsigned spin = 0; spin < GSPIN; ++spin) {
        const unsigned mask = lds_ld(&L.qrdy);
        WAIT_LKM0();
        a0 = ld_sc1(wsr, o0); a1 = ld_sc1(wsr, o1);
        WAIT_VM0();
        if (spin == 0 && lane == 0) __hip_atomic_fetch_add(&L.qpub, 1u, __ATOMIC_RELAXED, __HIP_MEMORY_SCOPE_WORKGROUP);
        const bool ok0 = (lsb4(a0) ^ t0) == 15u || ((mask >> p) & 1u);
        const bool ok1 = (lsb4(a1) ^ t1) == 15u || ((mask >> (p + 4)) & 1u);
        if (__all(ok0 && ok1)) break;
        __builtin_amdgcn_s_sleep(1);
      }
      float q[4];
#pragma unroll
      for (int k = 0; k < 4; ++k) { float t = __uint_as_float(a0[k] & ~1u) + __uint_as_float(a1[k] & ~1u); t = tr16(t, t); q[k] = tr32(t, t); }
      if (p == 0) {
        bf16x2 lo = {(__bf16)q[0], (__bf16)q[1]}, hi = {(__bf16)q[2], (__bf16)q[3]};
        *(u32x2*)&L.qs[j * 32 + c4 * 2] = u32x2{__builtin_bit_cast(unsigned, lo), __builtin_bit_cast(unsigned, hi)};
      }
      WAIT_LKM0();
      if (lane == 0) __hip_atomic_fetch_add(&L.qcnt, 1u, __ATOMIC_RELAXED, __HIP_MEMORY_SCOPE_WORKGROUP);
      STAMP(2);
    } else if (j == 4) {
      const bool act = lane < 40;
      const unsigned mine = act ? WS_HS + b * 512 + lane * 8 : OOB;
      const unsigned theirs = !act ? OOB : lane < 8 ? WS_HS + (h * 8 + lane) * 512 + s * 8 : WS_HS + ((lane - 8) * 8 + s) * 512 + (8 + h) * 8;
      u32x2 r = ld8_sc1(wsr, theirs);
      const u32x4 sv = {SENT, SENT, SENT, SENT};
      st_sc1(wsr, WS_P1 + b * 1024 + lane * 16, sv);
      st_sc1(wsr, b < 160 ? WS_P2 + b * 2048 + lane * 16 : OOB, sv);
      st_sc1(wsr, b < 160 ? WS_P2 + b * 2048 + 1024 + lane * 16 : OOB, sv);
      {
        const int u = lane < 16 ? b : 256 + (b >> 1);
        const bool on = lane < 16 || (lane < 32 && !(b & 1));
        const int mat = u / 192, lb = (u % 192) >> 3, rp = u & 7;
        st_sc1(wsr, on ? WS_FO + ((mat * 8 + rp) * 1536 + lb * 64 + (lane & 15) * 4) * 4 : OOB, sv);
      }
      __builtin_amdgcn_raw_buffer_store_b32(0u, wsr, (s == 0 && lane == 0) ? WS_CNT + h * 256 : OOB, 0, AUX_SC1);
      __builtin_amdgcn_sched_barrier(0);
      __builtin_amdgcn_s_barrier();
      WAIT_VM0();
      const unsigned n = (r.y + 1u) & 0x3fffffffu;
      unsigned echo = r.x & 0x3fffffffu, flags = 0;
      st8_sc1(wsr, mine, u32x2{n, echo});
      bool done = false;
#pragma nounroll
      for (unsigned spin = 0; spin < GSPIN; ++spin) {
        asm volatile("" ::: "memory");
        r = ld8_sc1(wsr, theirs);
        const unsigned qp = lds_ld(&L.qpub);
        const bool fresh = r.y == n;
        const unsigned long long fr = __ballot(fresh && act);
        const bool st1 = (fr & 0xffull) == 0xffull;
        const unsigned qm = (unsigned)__ballot(fresh && lane < 8 && ((r.x >> 30) & 1u));
        if (lane == 0) lds_st(&L.qrdy, qm);
        const unsigned nf = lane < 8 ? (qp >= 3u ? 1u << 30 : 0u) : (st1 ? 1u << 31 : 0u);
        const unsigned pn = r.x & 0x3fffffffu;
        const bool upd = pn != echo || nf != flags;
        echo = pn; flags = nf;
        st8_sc1(wsr, upd ? mine : OOB, u32x2{n | flags, echo});
        const unsigned long long cf = __ballot(fresh && act && lane >= 8 && (r.x >> 31));
        if (!done && st1 && cf == 0xffffffff00ull) { done = true; if (lane == 0) lds_st(&L.bdone, 1u); STAMP(1); }
        if (done && qp >= 3u && qm == 0xffu) break;
        __builtin_amdgcn_s_sleep(4);
      }
      STAMP(2);
    } else {
      __builtin_amdgcn_s_barrier();
    }
    lds_wait_ge<2>(&L.qcnt, 3u);
    const unsigned f0 = (h * 4 + rq) * 12;
    if (j >= 3) {
      const int unit = j == 3 ? b : ((b & 1) ? -1 : 256 + (b >> 1));
      const int u = unit < 0 ? 0 : unit;
      const int mat = u / 192, lb = (u % 192) >> 3, rp = u & 7;
      GemvLoads g;
      gemv_issue(g, xr, mkrsrc(mat ? w3 : w1, unit < 0 ? 0 : D * F * 2), F * 2, rp * 80, lb * 128, lane);
      float pa[4];
      gemv_compute(g, pa);
      lds_wait_ge<8>(&L.bdone, 1u);
      st_sc1(wsr, (wr16 && unit >= 0) ? WS_FO + ((mat * 8 + rp) * 1536 + lb * 64 + col4) * 4 : OOB, u32x4{san(pa[0]), san(pa[1]), san(pa[2]), san(pa[3])});
    }
    const u32x4 gxa = __builtin_amdgcn_raw_buffer_load_b128(xr, lane * 20, 0, 0);
    const unsigned gxb = __builtin_amdgcn_raw_buffer_load_b32(xr, lane * 20 + 16, 0, 0);
    const rsrc_t gr = mkrsrc(gw, D * 2);
    const u32x4 gwa = __builtin_amdgcn_raw_buffer_load_b128(gr, lane * 20, 0, 0);
    const unsigned gwb = __builtin_amdgcn_raw_buffer_load_b32(gr, lane * 20 + 16, 0, 0);
    u32x4 w2t[2];
    const unsigned colB = (half * 320 + j * 64 + c * 8) * 2;
    {
      const rsrc_t wor = mkrsrc(wo, H * DH * D * 2);
      const int cs = rq * 2 + (rg >> 2), i0 = (rg & 3) * 6;
#pragma unroll
      for (int t = 0; t < 6; ++t) { const int i = i0 + t; const unsigned d = (i >> 3) * 64 + cs * 8 + (i & 7); wot[t] = ld_nt(wor, (h * DH + d) * (D * 2) + colB, 0); }
      const rsrc_t w2r = mkrsrc(w2, F * D * 2);
      w2t[0] = ld_nt(w2r, (f0 + rg) * (D * 2) + colB, 0);
      w2t[1] = ld_nt(w2r, rg < 4 ? (f0 + 8 + rg) * (D * 2) + colB : OOB, 0);
    }
    {
      float z = 0.f;
#pragma unroll
      for (int k = 0; k < 4; ++k) { z = fmaf(bf_lo(gxa[k]), bf_lo(gwa[k]), z); z = fmaf(bf_hi(gxa[k]), bf_hi(gwa[k]), z); }
      z = fmaf(bf_lo(gxb), bf_lo(gwb), z); z = fmaf(bf_hi(gxb), bf_hi(gwb), z);
      z = sum_hi3(sum_lo3(z));
      gate = 1.f / (1.f + __expf(-z));
    }
    if (j == 3) {
      const int part = lane >> 4, mr = lane & 15;
      const unsigned o = part < 3 ? WS_FO + (mr * 1536 + f0 + part * 4) * 4 : OOB;
      u32x4 a;
#pragma nounroll
      for (unsigned spin = 0; spin < GSPIN; ++spin) {
        asm volatile("" ::: "memory");
        a = ld_sc1(wsr, o);
        if (__all(valid4(a))) break;
        __builtin_amdgcn_s_sleep(16);
      }
      float hv[4];
#pragma unroll
      for (int k = 0; k < 4; ++k) {
        const float t = sum_lo3(__uint_as_float(a[k]));
        const float o8 = dpp<XOR8>(t);
        const float gv = (mr & 8) ? o8 : t, uv = (mr & 8) ? t : o8;
        hv[k] = gv / (1.f + __expf(-gv)) * uv;
      }
      if (mr == 0) *(f32x4*)&L.hs[part * 4] = f32x4{hv[0], hv[1], hv[2], hv[3]};
      WAIT_LKM0();
      if (lane == 0) lds_st(&L.hrdy, 1u);
    }
    lds_wait_ge<8>(&L.hrdy, 1u);
    {
      const float h0 = L.hs[rg] * (1.f - gate), h1 = L.hs[8 + rg] * (1.f - gate);
#pragma unroll
      for (int k = 0; k < 4; ++k) {
        y[2 * k] = h0 * bf_lo(w2t[0][k]) + h1 * bf_lo(w2t[1][k]);
        y[2 * k + 1] = h0 * bf_hi(w2t[0][k]) + h1 * bf_hi(w2t[1][k]);
      }
    }
    STAMP(3);
  } else {
    const int sw = wave - NHELP;
    const unsigned long long kb = (unsigned long long)(kc + (size_t)h * SK * DH), vb = (unsigned long long)(vc + (size_t)h * SK * DH);
    const u32x4 kr = {(unsigned)kb, (unsigned)(kb >> 32) & 0xffffu, (unsigned)(SK * DH * 2), 0x00020000u};
    const u32x4 vr = {(unsigned)vb, (unsigned)(vb >> 32) & 0xffffu, (unsigned)(SK * DH * 2), 0x00020000u};
    const unsigned loff = (unsigned)(rg * DH + c * 8) * 2;
    constexpr unsigned STEPB = 8 * DH * 2;
    constexpr int NWH = 8 * NSTR;
    constexpr int NS = DYN ? NSTAT : 1024 / NWH;
    constexpr unsigned STAT = NS * NWH, NDYN = 1024 - STAT;
    static_assert(NS % RING == 0 && NS >= 2 * RING && AHEAD < RING && (DYN || NDYN == 0), "");
    const unsigned wid = s * NSTR + sw;
    u32x4 K[RING][3], V[RING][3];
#define RING_LOADS "s_nop 4\n\t" \
                   "buffer_load_dwordx4 %0, %6, %7, %9 offen nt\n\t" \
                   "buffer_load_dwordx4 %1, %6, %7, %9 offen offset:128 nt\n\t" \
                   "buffer_load_dwordx4 %2, %6, %7, %9 offen offset:256 nt\n\t" \
                   "buffer_load_dwordx4 %3, %6, %8, %9 offen nt\n\t" \
                   "buffer_load_dwordx4 %4, %6, %8, %9 offen offset:128 nt\n\t" \
                   "buffer_load_dwordx4 %5, %6, %8, %9 offen offset:256 nt"
    auto issue0 = [&](int slot, unsigned unit) {
      const unsigned so = unit * STEPB;
      asm volatile(RING_LOADS
                   : "=&v"(K[slot][0]), "=&v"(K[slot][1]), "=&v"(K[slot][2]), "=&v"(V[slot][0]), "=&v"(V[slot][1]), "=&v"(V[slot][2])
                   : "v"(loff), "s"(kr), "s"(vr), "s"(so));
    };
    auto issue = [&](int slot, unsigned unit) {
      const unsigned so = unit * STEPB;
      asm volatile(RING_LOADS
                   : "+v"(K[slot][0]), "+v"(K[slot][1]), "+v"(K[slot][2]), "+v"(V[slot][0]), "+v"(V[slot][1]), "+v"(V[slot][2])
                   : "v"(loff), "s"(kr), "s"(vr), "s"(so));
    };
    __builtin_amdgcn_s_barrier();
    __builtin_amdgcn_sched_barrier(0);
#pragma unroll
    for (int i = 0; i < RING; ++i) {
      if (i >= INF) asm volatile("s_waitcnt vmcnt(%0)" :: "i"((INF - 1) * 6) : "memory");
      issue0(i, i * NWH + wid);
      __builtin_amdgcn_sched_barrier(0);
    }
    STAMP(1);
    constexpr float C2 = 0.07216878364870322f * 1.4426950408889634f;
    u32x4 qp[3];
    auto consume = [&](int slot, auto younger) {
      constexpr int YN = decltype(younger)::value * 6;
      asm volatile("s_waitcnt vmcnt(%3)" : "+v"(K[slot][0]), "+v"(K[slot][1]), "+v"(K[slot][2]) : "i"(YN + 3));
      float d = 0.f;
#pragma unroll
      for (int jj = 0; jj < 3; ++jj)
#pragma unroll
        for (int k = 0; k < 4; ++k) d = dot2(K[slot][jj][k], qp[jj][k], d);
      const float sc = sum_lo3(d) * C2;
      float pm = fmaxf(sc, dpp<XOR8>(sc)); pm = max32(max16(pm));
      const float mn = fmaxf(m, pm);
      const float alpha = __builtin_amdgcn_exp2f(m - mn), p = __builtin_amdgcn_exp2f(sc - mn);
      l = fmaf(l, alpha, p); m = mn;
      asm volatile("s_waitcnt vmcnt(%3)" : "+v"(V[slot][0]), "+v"(V[slot][1]), "+v"(V[slot][2]) : "i"(YN));
#pragma unroll
      for (int jj = 0; jj < 3; ++jj)
#pragma unroll
        for (int k = 0; k < 4; ++k) {
          acc[jj * 8 + 2 * k] = fmaf(p, bf_lo(V[slot][jj][k]), acc[jj * 8 + 2 * k] * alpha);
          acc[jj * 8 + 2 * k + 1] = fmaf(p, bf_hi(V[slot][jj][k]), acc[jj * 8 + 2 * k + 1] * alpha);
        }
#pragma unroll
      for (int jj = 0; jj < 3; ++jj)
        asm volatile("" : "+v"(acc[jj * 8]), "+v"(acc[jj * 8 + 1]), "+v"(acc[jj * 8 + 2]), "+v"(acc[jj * 8 + 3]), "+v"(acc[jj * 8 + 4]), "+v"(acc[jj * 8 + 5]), "+v"(acc[jj * 8 + 6]), "+v"(acc[jj * 8 + 7]));
    };
    {
      {
        lds_wait_ge<1>(&L.qcnt, 3u);
#pragma unroll
        for (int jj = 0; jj < 3; ++jj) qp[jj] = *(const u32x4*)&L.qs[jj * 32 + c * 4];
        STAMP(2);
        static_for<0, RING>([&](auto ic) {
          constexpr int i = decltype(ic)::value;
          if constexpr (i + AHEAD >= RING) issue((i + AHEAD) % RING, (i + AHEAD) * NWH + wid);
          consume(i, IC<(i + AHEAD >= RING ? AHEAD : RING - 1 - i)>{});
          __builtin_amdgcn_sched_barrier(0);
        });
#pragma nounroll
        for (int i0 = RING; i0 < NS - RING; i0 += RING) {
#pragma unroll
          for (int k = 0; k < RING; ++k) {
            issue((k + AHEAD) % RING, (i0 + k + AHEAD) * NWH + wid);
            consume(k, IC<AHEAD>{});
            __builtin_amdgcn_sched_barrier(0);
          }
        }
        if constexpr (!DYN) {
          static_for<0, RING>([&](auto ic) {
            constexpr int k = decltype(ic)::value;
            if constexpr (k + AHEAD < RING) issue((k + AHEAD) % RING, (NS - RING + k + AHEAD) * NWH + wid);
            consume(k, IC<(k + AHEAD < RING ? AHEAD : RING - 1 - k)>{});
            __builtin_amdgcn_sched_barrier(0);
          });
        } else {
#pragma unroll
          for (int k = 0; k < RING - AHEAD; ++k) {
            issue((k + AHEAD) % RING, (NS - RING + k + AHEAD) * NWH + wid);
            consume(k, IC<AHEAD>{});
            __builtin_amdgcn_sched_barrier(0);
          }
          int rem = AHEAD;
          bool more = true;
#pragma nounroll
          do {
#pragma unroll
            for (int k = 0; k < RING; ++k) {
              if (rem > 0) {
                unsigned t = NDYN;
                if (more) {
                  unsigned idx = 0;
                  if (lane == 0) idx = __hip_atomic_fetch_add(&L.tcons, 1u, __ATOMIC_RELAXED, __HIP_MEMORY_SCOPE_WORKGROUP);
                  idx = __builtin_amdgcn_readfirstlane(idx);
#pragma nounroll
                  for (unsigned spin = 0; spin < LSPIN; ++spin) {
                    const unsigned pr = __builtin_amdgcn_readfirstlane(lds_ld(&L.tprod));
                    if (idx < (pr & 0x7fffffffu)) { asm volatile("" ::: "memory"); t = __builtin_amdgcn_readfirstlane(lds_ld(&L.tq[idx])); break; }
                    if (pr >> 31) break;
                    __builtin_amdgcn_s_sleep(1);
                  }
                }
                more = t < NDYN;
                issue(k, STAT + (more ? t : NDYN));
                rem += more ? 0 : -1;
                consume((RING - AHEAD + k) % RING, IC<AHEAD>{});
                __builtin_amdgcn_sched_barrier(0);
              }
            }
          } while (rem > 0);
#pragma unroll
          for (int r2 = 0; r2 < RING; ++r2)
            asm volatile("s_waitcnt vmcnt(0)" :: "v"(K[r2][0]), "v"(K[r2][1]), "v"(K[r2][2]), "v"(V[r2][0]), "v"(V[r2][1]), "v"(V[r2][2]));
          __builtin_amdgcn_sched_barrier(0);
        }
        STAMP(3);
      }
    }
  }
  int wave2 = wave;
  asm volatile("" : "+s"(wave2));
  const int mrow = wave2 >= NHELP ? wave2 - NHELP : ((DRY && wave2 == 2) ? NSTR : -1);
  if (mrow >= 0) {
    {
      const bool real = mrow < NSTR;
      float r6[6];
      {
        float r12[12];
#pragma unroll
        for (int i = 0; i < 12; ++i) r12[i] = tr32(acc[i], acc[i + 12]);
#pragma unroll
        for (int i = 0; i < 6; ++i) { const float t = tr16(r12[i], r12[i + 6]); r6[i] = t + dpp<XOR8>(t); }
      }
      const float lw = sum_hi3(l);
      if ((lane & 8) == 0) {
        const int slot = c * 4 + (lane >> 4);
        *(f32x4*)&L.mrg[mrow][slot * 8] = f32x4{r6[0], r6[1], r6[2], r6[3]};
        *(f32x2*)&L.mrg[mrow][slot * 8 + 4] = f32x2{r6[4], r6[5]};
      } else if (lane == 8) *(f32x2*)&L.ml[mrow][0] = f32x2{m, lw};
      WAIT_LKM0();
      unsigned old = 0;
      if (lane == 0) old = __hip_atomic_fetch_add(real ? &L.scount : &L.dummy, 1u, __ATOMIC_RELAXED, __HIP_MEMORY_SCOPE_WORKGROUP);
      old = __builtin_amdgcn_readfirstlane(old);
      if (!real || old == NSTR - 1) {
        asm volatile("" ::: "memory");
        const int hf = lane & 1;
        f32x4 pv[NSTR]; f32x2 pm[NSTR];
#pragma unroll
        for (int q2 = 0; q2 < NSTR; ++q2) { pv[q2] = *(const f32x4*)&L.mrg[q2][lane * 4]; pm[q2] = *(const f32x2*)&L.ml[q2][0]; }
        float MM = pm[0].x;
#pragma unroll
        for (int q2 = 1; q2 < NSTR; ++q2) MM = fmaxf(MM, pm[q2].x);
        float o0 = 0.f, o1 = 0.f, o2 = 0.f, o3 = 0.f;
#pragma unroll
        for (int q2 = 0; q2 < NSTR; ++q2) {
          const float ww = __builtin_amdgcn_exp2f(pm[q2].x - MM);
          o0 = fmaf(ww, pv[q2].x, o0); o1 = fmaf(ww, pv[q2].y, o1);
          o2 = fmaf(ww, pv[q2].z, o2); o3 = fmaf(ww, hf ? pm[q2].y : pv[q2].w, o3);
        }
        if (hf) o2 = MM;
        if (real) lds_wait_ge<1>(&L.bdone, 1u);
        st_sc1(wsr, real ? WS_P1 + b * 1024 + lane * 16 : OOB, u32x4{san(o0), san(o1), san(o2), san(o3)});
        if (real) STAMP(4);
      }
    }
  }
  int wave3 = wave;
  asm volatile("" : "+s"(wave3));
  if (wave3 < NHELP) {
#pragma nounroll
    for (int pass = 0; pass < npass; ++pass) {
      const bool real = pass == npass - 1;
      if (DYN && real && j == 2) {
        constexpr unsigned NDYN2 = 1024 - NSTAT * 8 * NSTR;
        lds_wait_ge<8>(&L.bdone, 1u);
        unsigned prod = 0;
#pragma nounroll
        for (unsigned spin = 0; spin < LSPIN; ++spin) {
          const unsigned need = __builtin_amdgcn_readfirstlane(lds_ld(&L.tcons)) + TQ - prod;
          if ((int)need > 0) {
            const unsigned base = __builtin_amdgcn_readfirstlane((unsigned)__builtin_amdgcn_raw_ptr_buffer_atomic_add_i32((int)need, wsr, (int)(lane == 0 ? WS_CNT + h * 256 : OOB), 0, 0));
            if ((unsigned)lane < need) L.tq[prod + lane] = base + lane;
            WAIT_LKM0();
            prod += need;
            const bool end = base + need >= NDYN2 || prod + TQ + NSTR > 320u;
            if (lane == 0) lds_st(&L.tprod, prod | (end ? 0x80000000u : 0u));
            if (end) break;
          } else __builtin_amdgcn_s_sleep(1);
        }
      }
      if (j == 0) {
        if (real) lds_wait_ge<1>(&L.scount, 1u);
        STAMP(4);
        const unsigned o = real ? WS_P1 + (h * 8 + c) * 1024 + (rq * 8 + rg) * 32 : OOB;
        u32x4 A, B;
#pragma nounroll
        for (unsigned spin = 0; spin < GSPIN; ++spin) {
          asm volatile("" ::: "memory");
          A = ld_sc1(wsr, o); B = ld_sc1(wsr, o + 16);
          if (__all(valid4(A) && valid4(B))) break;
        }
        STAMP(5);
        const float mp = __uint_as_float(B.z);
        const float M = max_lo3(mp);
        const float w = __builtin_amdgcn_exp2f(mp - M);
        const float sc = gate * __builtin_amdgcn_rcpf(sum_lo3(w * __uint_as_float(B.w)));
        const float o0 = sum_lo3(w * __uint_as_float(A.x)) * sc, o1 = sum_lo3(w * __uint_as_float(A.y)) * sc, o2 = sum_lo3(w * __uint_as_float(A.z)) * sc;
        const float o3 = sum_lo3(w * __uint_as_float(A.w)) * sc, o4 = sum_lo3(w * __uint_as_float(B.x)) * sc, o5 = sum_lo3(w * __uint_as_float(B.y)) * sc;
        if (c == 0) { *(f32x4*)&L.os[rg * 8] = f32x4{o0, o1, o2, o3}; *(f32x2*)&L.os[rg * 8 + 4] = f32x2{o4, o5}; }
        WAIT_LKM0();
        if (lane == 0) lds_st(&L.oflag, (unsigned)pass + 1u);
      } else {
        if (real) lds_wait_ge<4>(&L.scount, 1u);
        lds_wait_ge<0>(&L.oflag, (unsigned)pass + 1u);
      }
      const f32x4 oa = *(const f32x4*)&L.os[rg * 8];
      const f32x2 ob = *(const f32x2*)&L.os[rg * 8 + 4];
      float yt[8];
#pragma unroll
      for (int k = 0; k < 8; ++k) yt[k] = y[k];
#pragma unroll
      for (int t = 0; t < 6; ++t) {
        const float ov = t < 4 ? oa[t] : ob[t - 4];
#pragma unroll
        for (int k = 0; k < 4; ++k) { yt[2 * k] = fmaf(ov, bf_lo(wot[t][k]), yt[2 * k]); yt[2 * k + 1] = fmaf(ov, bf_hi(wot[t][k]), yt[2 * k + 1]); }
      }
      float r4[4];
#pragma unroll
      for (int k = 0; k < 4; ++k) { float t = tr32(yt[k], yt[k + 4]); t = tr16(t, t); r4[k] = t + dpp<XOR8>(t); }
      {
        const unsigned k = half * 80 + j * 16 + (col4 >> 2);
        st_sc1(wsr, (wr16 && real) ? WS_P2 + k * 2048 + (h * 4 + rq) * 16 : OOB, u32x4{san(r4[0]), san(r4[1]), san(r4[2]), san(r4[3])});
      }
      STAMP(6);
      if (j == 1 && b < 160) {
        const unsigned o0 = real ? WS_P2 + b * 2048 + lane * 16 : OOB;
        u32x4 a0, a1;
#pragma nounroll
        for (unsigned spin = 0; spin < GSPIN; ++spin) {
          asm volatile("" ::: "memory");
          a0 = ld_sc1(wsr, o0); a1 = ld_sc1(wsr, o0 + 1024);
          if (__all(valid4(a0) && valid4(a1))) break;
        }
        const float v0 = __uint_as_float(a0.x) + __uint_as_float(a1.x), v1 = __uint_as_float(a0.y) + __uint_as_float(a1.y);
        const float v2 = __uint_as_float(a0.z) + __uint_as_float(a1.z), v3 = __uint_as_float(a0.w) + __uint_as_float(a1.w);
        float t = tr16(tr32(v0, v2), tr32(v1, v3));
        t += dpp<XOR8>(t); t = sum_lo3(t);
        const __bf16 ov = (__bf16)t;
        __builtin_amdgcn_raw_buffer_store_b16(__builtin_bit_cast(unsigned short, ov), mkrsrc(out, D * 2), ((lane & 15) == 0 && real) ? (b * 4 + (lane >> 4)) * 2 : OOB, 0, 0);
        STAMP(7);
      }
    }
  }
  STAMP_FLUSH();
}

extern "C" void kernel_launch(void* const* d_in, const int* in_sizes, int n_in, void* d_out, int out_size, void* d_ws, size_t ws_size,
                              hipStream_t stream) {
  fused_kernel<<<256, NTHR, 0, stream>>>((const uint16_t*)d_in[0], (const uint16_t*)d_in[1], (const uint16_t*)d_in[2], (const uint16_t*)d_in[3],
                                         (const uint16_t*)d_in[4], (const uint16_t*)d_in[6], (const uint16_t*)d_in[7], (const uint16_t*)d_in[8],
                                         (const uint16_t*)d_in[9], (unsigned char*)d_ws, (uint16_t*)d_out);
}
```

```cpp
#include <hip/hip_runtime.h>
#include <hip/hip_bf16.h>
#include <stdint.h>
#include <stddef.h>

typedef unsigned u32x4 __attribute__((ext_vector_type(4)));
typedef unsigned u32x2 __attribute__((ext_vector_type(2)));
typedef float f32x4 __attribute__((ext_vector_type(4)));
typedef float f32x2 __attribute__((ext_vector_type(2)));
typedef __bf16 bf16x2 __attribute__((ext_vector_type(2)));
typedef __amdgpu_buffer_rsrc_t rsrc_t;

#define NSTR 4
#define RING 4
#define AHEAD 2
#define INF 2
#define NDYNU 192
#define TQ 16
#define TQMIN 4
#define EARLY 2
#define PMAX 6
#define QDELAY 8
#define QSLEEP 2
#define HSSLEEP 16
#define FFN_T 2000
#define ROWMAP 1
#define FOPM 1
#define P2PM 0
#define TILE_T 0
#define TBATCH 1
#define XCHK 24
#define RATE 0.05f
#define DRY 1

constexpr int D = 640, H = 32, SK = 8192, DH = 192, F = 1536;
constexpr int NHELP = 5, NWAVES = NHELP + NSTR, NTHR = NWAVES * 64;
constexpr int AUX_NT = 2, AUX_SC1 = 16;
constexpr unsigned OOB = 0x7ffffff0u;
constexpr unsigned WS_QO = 0;
constexpr unsigned WS_FO = WS_QO + 256 * 192 * 4;
constexpr unsigned WS_P1 = WS_FO + 128 * 768;
constexpr unsigned WS_P2 = WS_P1 + 256 * 1024;
constexpr unsigned WS_TG = WS_P2 + 160 * 2048;
constexpr unsigned TGS = 128;
constexpr unsigned WS_CNT = WS_TG + 256 * TGS;
constexpr unsigned WS_END = WS_CNT + 32 * 256;
constexpr unsigned WS_STAMP = 2u << 20;
constexpr unsigned SENT = 0xFFFFFFFFu;
constexpr unsigned NWH = 8 * NSTR, NDYN = NDYNU, STAT = 1024 - NDYN, UPW = STAT / 8;
constexpr unsigned R0 = RING + PMAX + AHEAD;
constexpr unsigned NSW = UPW - R0 * NSTR;
static_assert(STAT % NWH == 0 && UPW >= R0 * NSTR && AHEAD < RING, "");

__device__ __forceinline__ float bf_lo(unsigned u) { return __builtin_bit_cast(float, u << 16); }
__device__ __forceinline__ float bf_hi(unsigned u) { return __builtin_bit_cast(float, u & 0xffff0000u); }
__device__ __forceinline__ float dot2(unsigned k, unsigned q, float acc) {
  return __builtin_amdgcn_fdot2_f32_bf16(__builtin_bit_cast(bf16x2, k), __builtin_bit_cast(bf16x2, q), acc, false);
}
template <int CTRL> __device__ __forceinline__ float dpp(float x) {
  return __builtin_bit_cast(float, __builtin_amdgcn_mov_dpp(__builtin_bit_cast(int, x), CTRL, 0xf, 0xf, true));
}
template <int CTRL> __device__ __forceinline__ unsigned udpp(unsigned x) { return (unsigned)__builtin_amdgcn_mov_dpp((int)x, CTRL, 0xf, 0xf, true); }
constexpr int XOR1 = 0xB1, XOR2 = 0x4E, HMIR = 0x141, XOR8 = 0x128;
__device__ __forceinline__ float tr16(float a, float b) {
  auto s = __builtin_amdgcn_permlane16_swap(__float_as_uint(a), __float_as_uint(b), false, false);
  return __uint_as_float(s[0]) + __uint_as_float(s[1]);
}
__device__ __forceinline__ float tr32(float a, float b) {
  auto s = __builtin_amdgcn_permlane32_swap(__float_as_uint(a), __float_as_uint(b), false, false);
  return __uint_as_float(s[0]) + __uint_as_float(s[1]);
}
__device__ __forceinline__ float max16(float x) {
  auto s = __builtin_amdgcn_permlane16_swap(__float_as_uint(x), __float_as_uint(x), false, false);
  return fmaxf(__uint_as_float(s[0]), __uint_as_float(s[1]));
}
__device__ __forceinline__ float max32(float x) {
  auto s = __builtin_amdgcn_permlane32_swap(__float_as_uint(x), __float_as_uint(x), false, false);
  return fmaxf(__uint_as_float(s[0]), __uint_as_float(s[1]));
}
__device__ __forceinline__ float sum_hi3(float x) { x += dpp<XOR8>(x); x = tr16(x, x); return tr32(x, x); }
__device__ __forceinline__ float sum_lo3(float x) { x += dpp<XOR1>(x); x += dpp<XOR2>(x); x += dpp<HMIR>(x); return x; }
__device__ __forceinline__ float max_lo3(float x) { x = fmaxf(x, dpp<XOR1>(x)); x = fmaxf(x, dpp<XOR2>(x)); return fmaxf(x, dpp<HMIR>(x)); }
__device__ __forceinline__ unsigned san(float v) { unsigned u = __float_as_uint(v); return u < 0xFFFFFFFEu ? u : 0xFFFFFFFEu; }
__device__ __forceinline__ rsrc_t mkrsrc(const void* p, unsigned bytes) { return __builtin_amdgcn_make_buffer_rsrc((void*)p, 0, bytes, 0x00020000); }
__device__ __forceinline__ u32x4 ld_nt(rsrc_t r, unsigned voff) { return __builtin_amdgcn_raw_buffer_load_b128(r, voff, 0, AUX_NT); }
__device__ __forceinline__ u32x4 ld_sc1(rsrc_t r, unsigned voff) { return __builtin_amdgcn_raw_buffer_load_b128(r, voff, 0, AUX_SC1); }
__device__ __forceinline__ unsigned ld4_sc1(rsrc_t r, unsigned voff) { return __builtin_amdgcn_raw_buffer_load_b32(r, voff, 0, AUX_SC1); }
__device__ __forceinline__ void st_sc1(rsrc_t r, unsigned voff, u32x4 v) { __builtin_amdgcn_raw_buffer_store_b128(v, r, voff, 0, AUX_SC1); }
__device__ __forceinline__ void st4_sc1(rsrc_t r, unsigned voff, unsigned v) { __builtin_amdgcn_raw_buffer_store_b32(v, r, voff, 0, AUX_SC1); }
__device__ __forceinline__ bool valid4(u32x4 v) { return v.x != SENT && v.y != SENT && v.z != SENT && v.w != SENT; }
__device__ __forceinline__ unsigned lsb4(u32x4 v) { return (v.x & 1u) | ((v.y & 1u) << 1) | ((v.z & 1u) << 2) | ((v.w & 1u) << 3); }
__device__ __forceinline__ unsigned long long rt() { return __builtin_amdgcn_s_memrealtime(); }
__device__ __forceinline__ unsigned lds_ld(const unsigned* p) { return __hip_atomic_load(p, __ATOMIC_RELAXED, __HIP_MEMORY_SCOPE_WORKGROUP); }
__device__ __forceinline__ void lds_st(unsigned* p, unsigned v) { __hip_atomic_store(p, v, __ATOMIC_RELAXED, __HIP_MEMORY_SCOPE_WORKGROUP); }
__device__ __forceinline__ unsigned lds_add(unsigned* p) { return __hip_atomic_fetch_add(p, 1u, __ATOMIC_RELAXED, __HIP_MEMORY_SCOPE_WORKGROUP); }
__device__ __forceinline__ unsigned rfl(unsigned v) { return __builtin_amdgcn_readfirstlane(v); }
constexpr unsigned GSPIN = 1u << 16;
constexpr unsigned LSPIN = 1u << 20;
template <int SLEEP> __device__ __forceinline__ void lds_wait_ge(const unsigned* p, unsigned v) {
#pragma nounroll
  for (unsigned spin = 0; spin < LSPIN; ++spin) {
    if (rfl(lds_ld(p)) >= v) break;
    __builtin_amdgcn_s_sleep(SLEEP);
  }
  asm volatile("" ::: "memory");
}
#define WAIT_VM0() asm volatile("s_waitcnt vmcnt(0)" ::: "memory")
#define WAIT_LKM0() asm volatile("s_waitcnt lgkmcnt(0)" ::: "memory")

#define STAMP(i) do {} while (0)
#define STAMP_FLUSH() do {} while (0)

template <int N> struct IC { static constexpr int value = N; };
template <int B, int E, class Fn> __device__ __forceinline__ void static_for(Fn&& f) { if constexpr (B < E) { f(IC<B>{}); static_for<B + 1, E>(f); } }

struct Lds {
  float mrg[NSTR + 1][256];
  float ml[8][2];
  unsigned qs[96];
  float os[64];
  float hs[16];
  unsigned qcnt;
  unsigned pre;
  unsigned qrdy;
  unsigned bdone;
  unsigned hrdy;
  unsigned oflag;
  unsigned dummy;
  unsigned tcons;
  unsigned tprod;
  unsigned pad[3];
  unsigned sflag[4];
  unsigned tsn[8];
  u32x4 park[NSTR][PMAX > 0 ? PMAX : 1][6][64];
};
static_assert(offsetof(Lds, sflag) % 16 == 0 && offsetof(Lds, os) % 16 == 0 && offsetof(Lds, qs) % 16 == 0 && offsetof(Lds, hs) % 16 == 0, "LDS alignment");

struct GemvLoads { u32x4 w[10]; unsigned xs; };
__device__ __forceinline__ void gemv_issue(GemvLoads& g, rsrc_t xr, rsrc_t wr, unsigned ldB, unsigned row0, unsigned colB, int lane) {
  const unsigned rg = lane >> 3, c = lane & 7;
  const unsigned off = (row0 + rg) * ldB + colB + c * 16;
#pragma unroll
  for (int i = 0; i < 10; ++i) g.w[i] = ld_nt(wr, off + i * 8 * ldB);
  g.xs = __builtin_amdgcn_raw_buffer_load_b32(xr, lane < 40 ? row0 * 2 + lane * 4 : OOB, 0, 0);
}
__device__ __forceinline__ void gemv_compute(const GemvLoads& g, float (&r)[4], int lane) {
  float acc[8];
#pragma unroll
  for (int k = 0; k < 8; ++k) acc[k] = 0.f;
  const int rg = lane >> 3;
#pragma unroll
  for (int i = 0; i < 10; ++i) {
    const unsigned xw = (unsigned)__builtin_amdgcn_ds_bpermute((i * 4 + (rg >> 1)) * 4, (int)g.xs);
    const float xv = (rg & 1) ? bf_hi(xw) : bf_lo(xw);
#pragma unroll
    for (int k = 0; k < 4; ++k) { acc[2 * k] = fmaf(xv, bf_lo(g.w[i][k]), acc[2 * k]); acc[2 * k + 1] = fmaf(xv, bf_hi(g.w[i][k]), acc[2 * k + 1]); }
  }
#pragma unroll
  for (int k = 0; k < 4; ++k) { float t = tr32(acc[k], acc[k + 4]); t = tr16(t, t); r[k] = t + dpp<XOR8>(t); }
}

__global__ void __launch_bounds__(NTHR) fused_kernel(const uint16_t* __restrict__ x, const uint16_t* __restrict__ kc, const uint16_t* __restrict__ vc,
                                                     const uint16_t* __restrict__ wq, const uint16_t* __restrict__ wo, const uint16_t* __restrict__ w1,
                                                     const uint16_t* __restrict__ w3, const uint16_t* __restrict__ w2, const uint16_t* __restrict__ gw,
                                                     unsigned char* __restrict__ ws, uint16_t* __restrict__ out) {
  __shared__ __attribute__((aligned(16))) Lds L;
  const int b = blockIdx.x, h = b >> 3, s = b & 7;
  const int tid = threadIdx.x, lane = tid & 63, wave = __builtin_amdgcn_readfirstlane(tid >> 6);
  const int rg = lane >> 3, c = lane & 7;
  STAMP(0);
  asm volatile("" :: "s"(x), "s"(kc), "s"(vc), "s"(wq), "s"(wo), "s"(w1), "s"(w3), "s"(w2), "s"(gw), "s"(ws), "s"(out));
  const rsrc_t wsr = mkrsrc(ws, WS_END);
  const rsrc_t xr = mkrsrc(x, D * 2);
  const int j = wave;
  const int rq = s >> 1, half = s & 1;
  const bool wr16 = (lane & 0x18) == 0;
  const unsigned col4 = c * 8 + (lane >> 5) * 4;
  const unsigned grp = h * 4 + rq;
  float gate = 0.f, y[8], m = -INFINITY, l = 0.f, acc[24];
  u32x4 wot[6], w2t[2];
#pragma unroll
  for (int i = 0; i < 24; ++i) acc[i] = 0.f;
#pragma unroll
  for (int i = 0; i < 8; ++i) y[i] = 0.f;
#pragma unroll
  for (int i = 0; i < 6; ++i) wot[i] = u32x4{0u, 0u, 0u, 0u};
  w2t[0] = w2t[1] = u32x4{0u, 0u, 0u, 0u};
  const unsigned long long tstart = rt();
  int npass = DRY ? 2 : 1;
  asm volatile("" : "+s"(npass));

  if (wave < NHELP) {
    const int unit = j == 3 ? b : ((b & 1) ? -1 : 256 + (b >> 1));
    const unsigned u = unit < 0 ? 0 : unit, mat = u / 192, lb = (u % 192) >> 3, rp = u & 7;
    const int p = lane >> 4, c4 = lane & 15;
    const unsigned o0 = WS_QO + ((h * 8 + p) * 192 + j * 64 + c4 * 4) * 4, o1 = o0 + 4 * 192 * 4;
    const unsigned om = wr16 ? WS_QO + (b * 192 + j * 64 + col4) * 4 : OOB;
    const unsigned tw = lane < 8 ? WS_TG + (h * 8 + lane) * TGS : (lane >= 32 && s == 0) ? WS_TG + (lane - 32) * 8 * TGS : OOB;
    auto load_tiles = [&]() {
      const unsigned colB = (half * 320 + j * 64 + c * 8) * 2;
      const rsrc_t wor = mkrsrc(wo, H * DH * D * 2);
      const int cs = rq * 2 + (rg >> 2), i0 = (rg & 3) * 6;
#pragma unroll
      for (int t = 0; t < 6; ++t) { const int i = i0 + t; const unsigned d = (i >> 3) * 64 + cs * 8 + (i & 7); wot[t] = ld_nt(wor, (h * DH + d) * (D * 2) + colB); }
      const rsrc_t w2r = mkrsrc(w2, F * D * 2);
      w2t[0] = ld_nt(w2r, (grp * 12 + rg) * (D * 2) + colB);
      w2t[1] = ld_nt(w2r, rg < 4 ? (grp * 12 + 8 + rg) * (D * 2) + colB : OOB);
    };
    GemvLoads g;
    float pa[4];
    if (j < 3) {
      const u32x4 sn0 = ld_sc1(wsr, o0), sn1 = ld_sc1(wsr, o1), snm = ld_sc1(wsr, om);
      gemv_issue(g, xr, mkrsrc(wq, D * H * DH * 2), H * DH * 2, s * 80, (h * DH + j * 64) * 2, lane);
      __builtin_amdgcn_sched_barrier(0);
      if (tid < 16) (&L.qcnt)[tid] = (tid >= 12 + NSTR) ? 1u : 0u;
      WAIT_LKM0();
      __builtin_amdgcn_s_barrier();
      __builtin_amdgcn_sched_barrier(0);
      if (EARLY == 2) load_tiles();
      __builtin_amdgcn_sched_barrier(0);
      const unsigned t0 = lsb4(sn0), t1 = lsb4(sn1);
      asm volatile("" :: "v"(t0), "v"(t1), "v"(snm.x) : "memory");
      if (lane == 0) lds_add(&L.pre);
      gemv_compute(g, pa, lane);
      STAMP(1);
      asm volatile("" :: "v"(pa[0]), "v"(pa[1]), "v"(pa[2]), "v"(pa[3]) : "memory");
      lds_wait_ge<0>(&L.pre, 4u);
      st_sc1(wsr, om, u32x4{(__float_as_uint(pa[0]) & ~1u) | (~snm.x & 1u), (__float_as_uint(pa[1]) & ~1u) | (~snm.y & 1u),
                            (__float_as_uint(pa[2]) & ~1u) | (~snm.z & 1u), (__float_as_uint(pa[3]) & ~1u) | (~snm.w & 1u)});
      if (EARLY == 1) load_tiles();
      u32x4 a0 = {0u, 0u, 0u, 0u}, a1 = {0u, 0u, 0u, 0u};
      {
        bool k0 = false, k1 = false;
        __builtin_amdgcn_s_sleep(QDELAY);
#pragma nounroll
        for (unsigned spin = 0; spin < GSPIN; ++spin) {
          const unsigned mask = lds_ld(&L.qrdy);
          WAIT_LKM0();
          const u32x4 n0 = ld_sc1(wsr, k0 ? OOB : o0), n1 = ld_sc1(wsr, k1 ? OOB : o1);
          WAIT_VM0();
          if (!k0) { a0 = n0; k0 = (lsb4(n0) ^ t0) == 15u || ((mask >> p) & 1u); }
          if (!k1) { a1 = n1; k1 = (lsb4(n1) ^ t1) == 15u || ((mask >> (p + 4)) & 1u); }
          if (__all(k0 && k1)) break;
          __builtin_amdgcn_s_sleep(QSLEEP);
        }
      }
      float q[4];
#pragma unroll
      for (int k = 0; k < 4; ++k) { float t = __uint_as_float(a0[k] & ~1u) + __uint_as_float(a1[k] & ~1u); t = tr16(t, t); q[k] = tr32(t, t); }
      if (p == 0) {
        bf16x2 lo = {(__bf16)q[0], (__bf16)q[1]}, hi = {(__bf16)q[2], (__bf16)q[3]};
        *(u32x2*)&L.qs[j * 32 + c4 * 2] = u32x2{__builtin_bit_cast(unsigned, lo), __builtin_bit_cast(unsigned, hi)};
      }
      asm volatile("" ::: "memory");
      if (lane == 0) lds_add(&L.qcnt);
      STAMP(2);
      if (!EARLY) { lds_wait_ge<2>(&L.qcnt, 3u); load_tiles(); }
      if (EARLY == 3) { while (rt() - tstart < TILE_T + 100 * j) __builtin_amdgcn_s_sleep(8); load_tiles(); }
    } else {
      unsigned snap = 0;
      if (j == 4) {
        const u32x2 snap2 = __builtin_amdgcn_raw_buffer_load_b64(wsr, tw, 0, AUX_SC1);
        snap = snap2.x;
        const u32x4 sv = {SENT, SENT, SENT, SENT};
        st_sc1(wsr, WS_P1 + b * 1024 + lane * 16, sv);
        st_sc1(wsr, b < 160 ? WS_P2 + b * 2048 + lane * 16 : OOB, sv);
        st_sc1(wsr, b < 160 ? WS_P2 + b * 2048 + 1024 + lane * 16 : OOB, sv);
        {
          const unsigned u2 = lane < 16 ? b : 256 + (b >> 1);
          const bool on = lane < 16 || (lane < 32 && !(b & 1));
          st_sc1(wsr, on ? WS_FO + (((u2 / 192) * 8 + (u2 & 7)) * 1536 + ((u2 % 192) >> 3) * 64 + (lane & 15) * 4) * 4 : OOB, sv);
        }
        __builtin_amdgcn_sched_barrier(0);
        __builtin_amdgcn_s_barrier();
        WAIT_VM0();
        if (lane < 8) L.tsn[lane] = snap2.y;
        asm volatile("" ::: "memory");
        if (lane == 0) lds_add(&L.pre);
      } else {
        __builtin_amdgcn_s_barrier();
      }
      if (!EARLY) lds_wait_ge<2>(&L.qcnt, 3u);
      gemv_issue(g, xr, mkrsrc(mat ? w3 : w1, unit < 0 ? 0 : D * F * 2), F * 2, rp * 80, lb * 128, lane);
      if (EARLY != 3) load_tiles();
      gemv_compute(g, pa, lane);
      if (j == 4) {
        const unsigned long long tl = rt();
#pragma nounroll
        for (unsigned spin = 0; spin < LSPIN; ++spin) {
          if (rfl(lds_ld(&L.qcnt)) >= 3u) break;
          __builtin_amdgcn_s_sleep(2);
          if (rt() - tl > 600ull && (spin & 15u) == 0u) {
            const unsigned cur = ld4_sc1(wsr, lane < 8 ? tw : OOB);
            const unsigned qm = (unsigned)__ballot(lane < 8 && ((cur ^ snap) & 3u));
            if (lane == 0) lds_st(&L.qrdy, qm);
          }
        }
        asm volatile("" ::: "memory");
        st4_sc1(wsr, lane == s ? tw : OOB, snap ^ 1u);
        STAMP(1);
        const unsigned want = s == 0 ? 3u : 4u;
        bool seen = s == 0 ? lane < 32 : lane != 0;
#pragma nounroll
        for (unsigned spin = 0; spin < GSPIN; ++spin) {
          __builtin_amdgcn_s_sleep(HSSLEEP);
          const unsigned cur = ld4_sc1(wsr, seen ? OOB : tw);
          seen = seen || ((cur ^ snap) & want) != 0u;
          if (__all(seen)) break;
        }
        st4_sc1(wsr, (s == 0 && lane == 0) ? tw : OOB, snap ^ 7u);
        if (lane == 0) lds_st(&L.bdone, 1u);
        STAMP(2);
      } else {
        lds_wait_ge<8>(&L.bdone, 1u);
      }
      const unsigned f = lb * 64 + col4, fg = f / 12u;
      st_sc1(wsr, (wr16 && unit >= 0) ? (FOPM ? WS_FO + ((mat * 8 + rp) * 1536 + f) * 4 : WS_FO + ((fg * 16 + mat * 8 + rp) * 12 + (f - fg * 12)) * 4) : OOB, u32x4{san(pa[0]), san(pa[1]), san(pa[2]), san(pa[3])});
      if (EARLY == 3) { while (rt() - tstart < TILE_T + 100 * j) __builtin_amdgcn_s_sleep(8); load_tiles(); }
    }
    {
      const u32x4 gxa = __builtin_amdgcn_raw_buffer_load_b128(xr, lane * 20, 0, 0);
      const unsigned gxb = __builtin_amdgcn_raw_buffer_load_b32(xr, lane * 20 + 16, 0, 0);
      const rsrc_t gr = mkrsrc(gw, D * 2);
      const u32x4 gwa = __builtin_amdgcn_raw_buffer_load_b128(gr, lane * 20, 0, 0);
      const unsigned gwb = __builtin_amdgcn_raw_buffer_load_b32(gr, lane * 20 + 16, 0, 0);
      float z = 0.f;
#pragma unroll
      for (int k = 0; k < 4; ++k) { z = fmaf(bf_lo(gxa[k]), bf_lo(gwa[k]), z); z = fmaf(bf_hi(gxa[k]), bf_hi(gwa[k]), z); }
      z = fmaf(bf_lo(gxb), bf_lo(gwb), z); z = fmaf(bf_hi(gxb), bf_hi(gwb), z);
      z = sum_hi3(sum_lo3(z));
      gate = 1.f / (1.f + __expf(-z));
    }
    if (NDYN > 0 && j == 2) {
      lds_wait_ge<8>(&L.tcons, XCHK);
      const unsigned old = L.tsn[lane & 7];
      const unsigned ta = WS_TG + (h * 8 + (lane & 7)) * TGS + 4;
      st4_sc1(wsr, lane == s ? ta : OOB, ((unsigned)rt() << 1) | (~old & 1u));
      unsigned tv = 0;
      bool seen = lane >= 8;
#pragma nounroll
      for (unsigned spin = 0; spin < GSPIN; ++spin) {
        __builtin_amdgcn_s_sleep(HSSLEEP);
        const unsigned cur = ld4_sc1(wsr, seen ? OOB : ta);
        if (!seen) { tv = cur; seen = ((cur ^ old) & 1u) != 0u; }
        if (__all(seen)) break;
      }
      const int rel = (int)((tv & ~1u) - (rfl(tv) & ~1u)) >> 1;
      const float lag = (float)rel - sum_lo3((float)rel) * 0.125f;
      float n = fmaxf((float)(NDYN / 8) - lag * RATE, 0.f);
      const float tot = sum_lo3(n);
      n *= (float)NDYN / tot;
      unsigned cum = (unsigned)(n * 16.f);
      { unsigned t = udpp<0x111>(cum); cum += (lane & 7) >= 1 ? t : 0u; t = udpp<0x112>(cum); cum += (lane & 7) >= 2 ? t : 0u; t = udpp<0x114>(cum); cum += (lane & 7) >= 4 ? t : 0u; }
      const unsigned hi = (lane & 7) == 7 ? NDYN : min((cum + 8u) >> 4, NDYN);
      const unsigned hprev = udpp<0x111>(hi);
      const unsigned lo = (lane & 7) == 0 ? 0u : hprev;
      if (lane == s) lds_st(&L.tprod, 0x80000000u | (lo << 12) | (hi - lo));
    }
  } else {
    const int sw = wave - NHELP;
    const unsigned long long kb = (unsigned long long)(kc + (size_t)h * SK * DH), vb = (unsigned long long)(vc + (size_t)h * SK * DH);
    const u32x4 kr = {(unsigned)kb, (unsigned)(kb >> 32) & 0xffffu, (unsigned)(SK * DH * 2), 0x00020000u};
    const u32x4 vr = {(unsigned)vb, (unsigned)(vb >> 32) & 0xffffu, (unsigned)(SK * DH * 2), 0x00020000u};
    const unsigned loff = (unsigned)(rg * DH + c * 8) * 2;
    constexpr unsigned STEPB = 8 * DH * 2;
    u32x4 K[RING][3], V[RING][3];
#define RING_LOADS "s_nop 4\n\t" \
                   "buffer_load_dwordx4 %0, %6, %7, %9 offen nt\n\t" \
                   "buffer_load_dwordx4 %1, %6, %7, %9 offen offset:128 nt\n\t" \
                   "buffer_load_dwordx4 %2, %6, %7, %9 offen offset:256 nt\n\t" \
                   "buffer_load_dwordx4 %3, %6, %8, %9 offen nt\n\t" \
                   "buffer_load_dwordx4 %4, %6, %8, %9 offen offset:128 nt\n\t" \
                   "buffer_load_dwordx4 %5, %6, %8, %9 offen offset:256 nt"
    auto issue0 = [&](int slot, unsigned unit) {
      const unsigned so = unit * STEPB;
      asm volatile(RING_LOADS
                   : "=&v"(K[slot][0]), "=&v"(K[slot][1]), "=&v"(K[slot][2]), "=&v"(V[slot][0]), "=&v"(V[slot][1]), "=&v"(V[slot][2])
                   : "v"(loff), "s"(kr), "s"(vr), "s"(so));
    };
    auto issue = [&](int slot, unsigned unit) {
      const unsigned so = unit * STEPB;
      asm volatile(RING_LOADS
                   : "+v"(K[slot][0]), "+v"(K[slot][1]), "+v"(K[slot][2]), "+v"(V[slot][0]), "+v"(V[slot][1]), "+v"(V[slot][2])
                   : "v"(loff), "s"(kr), "s"(vr), "s"(so));
    };
    __builtin_amdgcn_s_barrier();
    __builtin_amdgcn_sched_barrier(0);
#pragma unroll
    for (int i = 0; i < RING; ++i) {
      if (i >= INF) asm volatile("s_waitcnt vmcnt(%0)" :: "i"((INF - 1) * 6) : "memory");
      issue0(i, i * NWH + s * NSTR + sw);
      __builtin_amdgcn_sched_barrier(0);
    }
    unsigned pi = RING;
    auto fixed_unit = [&]() -> unsigned { return (pi++) * NWH + s * NSTR + sw; };
    int np = 0;
    if (PMAX > 0) {
      const unsigned pbase = rfl((unsigned)(size_t)&L.park[sw][0][0][0]);
#pragma nounroll
      while (np < PMAX) {
        asm volatile("s_waitcnt vmcnt(%0)" :: "i"((INF - 1) * 6) : "memory");
        if (rfl(lds_ld(&L.qcnt)) >= 3u) break;
        const unsigned so = fixed_unit() * STEPB, mb = pbase + np * 6144;
        unsigned keep;
        asm volatile("s_mov_b32 %0, m0\n\t"
                     "s_mov_b32 m0, %1\n\ts_nop 4\n\t"
                     "buffer_load_dwordx4 %2, %3, %5 offen nt lds\n\t"
                     "s_add_u32 m0, %1, 896\n\ts_nop 0\n\t"
                     "buffer_load_dwordx4 %2, %3, %5 offen offset:128 nt lds\n\t"
                     "s_add_u32 m0, %1, 1792\n\ts_nop 0\n\t"
                     "buffer_load_dwordx4 %2, %3, %5 offen offset:256 nt lds\n\t"
                     "s_add_u32 m0, %1, 3072\n\ts_nop 0\n\t"
                     "buffer_load_dwordx4 %2, %4, %5 offen nt lds\n\t"
                     "s_add_u32 m0, %1, 3968\n\ts_nop 0\n\t"
                     "buffer_load_dwordx4 %2, %4, %5 offen offset:128 nt lds\n\t"
                     "s_add_u32 m0, %1, 4864\n\ts_nop 0\n\t"
                     "buffer_load_dwordx4 %2, %4, %5 offen offset:256 nt lds\n\t"
                     "s_mov_b32 m0, %0"
                     : "=&s"(keep) : "s"(mb), "v"(loff), "s"(kr), "s"(vr), "s"(so) : "scc", "memory");
        ++np;
      }
    }
    STAMP(1);
    constexpr float C2 = 0.07216878364870322f * 1.4426950408889634f;
    u32x4 qp[3];
    float p_, alpha_;
    auto scores = [&](const u32x4 (&Kx)[3]) {
      float d = 0.f;
#pragma unroll
      for (int jj = 0; jj < 3; ++jj)
#pragma unroll
        for (int k = 0; k < 4; ++k) d = dot2(Kx[jj][k], qp[jj][k], d);
      const float sc = sum_lo3(d) * C2;
      float pm = fmaxf(sc, dpp<XOR8>(sc)); pm = max32(max16(pm));
      const float mn = fmaxf(m, pm);
      alpha_ = __builtin_amdgcn_exp2f(m - mn); p_ = __builtin_amdgcn_exp2f(sc - mn);
      l = fmaf(l, alpha_, p_); m = mn;
    };
    auto accum = [&](const u32x4 (&Vx)[3]) {
#pragma unroll
      for (int jj = 0; jj < 3; ++jj)
#pragma unroll
        for (int k = 0; k < 4; ++k) {
          acc[jj * 8 + 2 * k] = fmaf(p_, bf_lo(Vx[jj][k]), acc[jj * 8 + 2 * k] * alpha_);
          acc[jj * 8 + 2 * k + 1] = fmaf(p_, bf_hi(Vx[jj][k]), acc[jj * 8 + 2 * k + 1] * alpha_);
        }
#pragma unroll
      for (int jj = 0; jj < 3; ++jj)
        asm volatile("" : "+v"(acc[jj * 8]), "+v"(acc[jj * 8 + 1]), "+v"(acc[jj * 8 + 2]), "+v"(acc[jj * 8 + 3]), "+v"(acc[jj * 8 + 4]), "+v"(acc[jj * 8 + 5]), "+v"(acc[jj * 8 + 6]), "+v"(acc[jj * 8 + 7]));
    };
    auto consume = [&](int slot, auto younger) {
      constexpr int YN = decltype(younger)::value * 6;
      asm volatile("s_waitcnt vmcnt(%3)" : "+v"(K[slot][0]), "+v"(K[slot][1]), "+v"(K[slot][2]) : "i"(YN + 3));
      scores(K[slot]);
      asm volatile("s_waitcnt vmcnt(%3)" : "+v"(V[slot][0]), "+v"(V[slot][1]), "+v"(V[slot][2]) : "i"(YN));
      accum(V[slot]);
    };
    auto consume_parked = [&](int pi) {
      u32x4 T[3];
#pragma unroll
      for (int jj = 0; jj < 3; ++jj) T[jj] = L.park[sw][pi][jj][lane];
      scores(T);
      __builtin_amdgcn_sched_barrier(0);
#pragma unroll
      for (int jj = 0; jj < 3; ++jj) T[jj] = L.park[sw][pi][3 + jj][lane];
      accum(T);
    };
    lds_wait_ge<1>(&L.qcnt, 3u);
#pragma unroll
    for (int jj = 0; jj < 3; ++jj) qp[jj] = *(const u32x4*)&L.qs[jj * 32 + c * 4];
    STAMP(2);
    static_for<0, RING>([&](auto ic) {
      constexpr int i = decltype(ic)::value;
      consume(i, IC<RING - 1 - i + (i < AHEAD ? i : AHEAD)>{});
      if constexpr (i < AHEAD) issue(i, fixed_unit());
      __builtin_amdgcn_sched_barrier(0);
    });
    int rem = AHEAD;
    bool more = true;
#pragma nounroll
    do {
#pragma unroll
      for (int k = 0; k < RING; ++k) {
        if (rem > 0) {
          unsigned un = 1024u;
          if (pi < R0) un = fixed_unit();
          else if (more) {
            unsigned idx = 0;
            if (lane == 0) idx = lds_add(&L.tcons);
            idx = rfl(idx);
            if (idx < NSW) un = (R0 + idx / NSTR) * NWH + s * NSTR + idx % NSTR;
            else if (NDYN > 0) {
              idx -= NSW;
#pragma nounroll
              for (unsigned spin = 0; spin < LSPIN; ++spin) {
                const unsigned pr = rfl(lds_ld(&L.tprod));
                if (pr >> 31) { if (idx < (pr & 0xfffu)) un = STAT + ((pr >> 12) & 0xfffu) + idx; break; }
                __builtin_amdgcn_s_sleep(1);
              }
            }
          }
          more = un < 1024u;
          issue((k + AHEAD) % RING, un);
          rem += more ? 0 : -1;
          consume(k, IC<AHEAD>{});
          __builtin_amdgcn_sched_barrier(0);
        }
      }
      if (PMAX > 0 && np > 0) { asm volatile("" ::: "memory"); --np; consume_parked(np); __builtin_amdgcn_sched_barrier(0); }
    } while (rem > 0 || np > 0);
#pragma unroll
    for (int r2 = 0; r2 < RING; ++r2)
      asm volatile("s_waitcnt vmcnt(0)" :: "v"(K[r2][0]), "v"(K[r2][1]), "v"(K[r2][2]), "v"(V[r2][0]), "v"(V[r2][1]), "v"(V[r2][2]));
    __builtin_amdgcn_sched_barrier(0);
    STAMP(3);
  }
  int wave2 = wave;
  asm volatile("" : "+s"(wave2));
  const int mrow = wave2 >= NHELP ? wave2 - NHELP : ((DRY && wave2 == 1) ? NSTR : -1);
  if (mrow >= 0) {
    const bool real = mrow < NSTR;
    float r6[6];
    {
      float r12[12];
#pragma unroll
      for (int i = 0; i < 12; ++i) r12[i] = tr32(acc[i], acc[i + 12]);
#pragma unroll
      for (int i = 0; i < 6; ++i) { const float t = tr16(r12[i], r12[i + 6]); r6[i] = t + dpp<XOR8>(t); }
    }
    const float lw = sum_hi3(l);
    if ((lane & 8) == 0) {
      const int slot = c * 4 + (lane >> 4);
      *(f32x4*)&L.mrg[mrow][slot * 8] = f32x4{r6[0], r6[1], r6[2], r6[3]};
      *(f32x2*)&L.mrg[mrow][slot * 8 + 4] = f32x2{r6[4], r6[5]};
    } else if (lane == 8) *(f32x2*)&L.ml[mrow][0] = f32x2{m, lw};
    asm volatile("" ::: "memory");
    if (lane == 0) lds_st(real ? &L.sflag[mrow] : &L.dummy, 1u);
    if (real) STAMP(4);
  }
  int wave3 = wave;
  asm volatile("" : "+s"(wave3));
  if (wave3 < NHELP) {
#pragma nounroll
    for (int pass = (NDYN > 0 && j == 2) ? npass - 1 : 0; pass < npass; ++pass) {
      const bool real = pass == npass - 1;
      if (real) {
        if (j == 3) {
          const int part = lane >> 4, mr = lane & 15;
          const unsigned o = part < 3 ? (FOPM ? WS_FO + (mr * 1536 + grp * 12 + part * 4) * 4 : WS_FO + grp * 768 + (mr * 12 + part * 4) * 4) : OOB;
#pragma nounroll
          while (rt() - tstart < FFN_T && !rfl(lds_ld(&L.sflag[0]))) __builtin_amdgcn_s_sleep(32);
          u32x4 a;
#pragma nounroll
          for (unsigned spin = 0; spin < GSPIN; ++spin) {
            asm volatile("" ::: "memory");
            a = ld_sc1(wsr, o);
            if (__all(valid4(a))) break;
            __builtin_amdgcn_s_sleep(16);
          }
          float hv[4];
#pragma unroll
          for (int k = 0; k < 4; ++k) {
            const float t = sum_lo3(__uint_as_float(a[k]));
            const float o8 = dpp<XOR8>(t);
            const float gv = (mr & 8) ? o8 : t, uv = (mr & 8) ? t : o8;
            hv[k] = gv / (1.f + __expf(-gv)) * uv;
          }
          if (mr == 0) *(f32x4*)&L.hs[part * 4] = f32x4{hv[0], hv[1], hv[2], hv[3]};
          asm volatile("" ::: "memory");
          if (lane == 0) lds_st(&L.hrdy, 1u);
        }
        lds_wait_ge<8>(&L.hrdy, 1u);
        const float h0 = L.hs[rg] * (1.f - gate), h1 = L.hs[8 + rg] * (1.f - gate);
#pragma unroll
        for (int k = 0; k < 4; ++k) {
          y[2 * k] = h0 * bf_lo(w2t[0][k]) + h1 * bf_lo(w2t[1][k]);
          y[2 * k + 1] = h0 * bf_hi(w2t[0][k]) + h1 * bf_hi(w2t[1][k]);
        }
        STAMP(3);
      }
      if (j == 0) {
        if (real) {
#pragma nounroll
          for (unsigned spin = 0; spin < LSPIN; ++spin) {
            const u32x4 f = *(volatile u32x4*)&L.sflag[0];
            if (rfl(f.x & f.y & f.z & f.w)) break;
            if (!rfl(f.x | f.y | f.z)) __builtin_amdgcn_s_sleep(2);
          }
          asm volatile("" ::: "memory");
        }
        STAMP(4);
        {
          const int hf = lane & 1;
          f32x4 pv[NSTR]; f32x2 pm[NSTR];
#pragma unroll
          for (int q2 = 0; q2 < NSTR; ++q2) { pv[q2] = *(const f32x4*)&L.mrg[q2][lane * 4]; pm[q2] = *(const f32x2*)&L.ml[q2][0]; }
          float MM = pm[0].x;
#pragma unroll
          for (int q2 = 1; q2 < NSTR; ++q2) MM = fmaxf(MM, pm[q2].x);
          float o0 = 0.f, o1 = 0.f, o2 = 0.f, o3 = 0.f;
#pragma unroll
          for (int q2 = 0; q2 < NSTR; ++q2) {
            const float ww = __builtin_amdgcn_exp2f(pm[q2].x - MM);
            o0 = fmaf(ww, pv[q2].x, o0); o1 = fmaf(ww, pv[q2].y, o1);
            o2 = fmaf(ww, pv[q2].z, o2); o3 = fmaf(ww, hf ? pm[q2].y : pv[q2].w, o3);
          }
          if (hf) o2 = MM;
          st_sc1(wsr, real ? WS_P1 + b * 1024 + lane * 16 : OOB, u32x4{san(o0), san(o1), san(o2), san(o3)});
        }
        STAMP(7);
        const unsigned o = real ? WS_P1 + (h * 8 + c) * 1024 + (rq * 8 + rg) * 32 : OOB;
        u32x4 A = {0u, 0u, 0u, 0u}, B = {0u, 0u, 0u, 0u};
        bool ok = false;
#pragma nounroll
        for (unsigned spin = 0; spin < GSPIN; ++spin) {
          asm volatile("" ::: "memory");
          const u32x4 nA = ld_sc1(wsr, ok ? OOB : o), nB = ld_sc1(wsr, ok ? OOB : o + 16);
          if (!ok) { A = nA; B = nB; ok = valid4(nA) && valid4(nB); }
          if (__all(ok)) break;
        }
        STAMP(5);
        const float mp = __uint_as_float(B.z);
        const float M = max_lo3(mp);
        const float w = __builtin_amdgcn_exp2f(mp - M);
        const float sc = gate * __builtin_amdgcn_rcpf(sum_lo3(w * __uint_as_float(B.w)));
        const float o0 = sum_lo3(w * __uint_as_float(A.x)) * sc, o1 = sum_lo3(w * __uint_as_float(A.y)) * sc, o2 = sum_lo3(w * __uint_as_float(A.z)) * sc;
        const float o3 = sum_lo3(w * __uint_as_float(A.w)) * sc, o4 = sum_lo3(w * __uint_as_float(B.x)) * sc, o5 = sum_lo3(w * __uint_as_float(B.y)) * sc;
        if (c == 0) { *(f32x4*)&L.os[rg * 8] = f32x4{o0, o1, o2, o3}; *(f32x2*)&L.os[rg * 8 + 4] = f32x2{o4, o5}; }
        asm volatile("" ::: "memory");
        if (lane == 0) lds_st(&L.oflag, (unsigned)pass + 1u);
        asm volatile("" ::: "memory");
      } else {
        if (real) lds_wait_ge<4>(&L.sflag[0], 1u);
        lds_wait_ge<0>(&L.oflag, (unsigned)pass + 1u);
      }
      const f32x4 oa = *(const f32x4*)&L.os[rg * 8];
      const f32x2 ob = *(const f32x2*)&L.os[rg * 8 + 4];
      float yt[8];
#pragma unroll
      for (int k = 0; k < 8; ++k) yt[k] = y[k];
#pragma unroll
      for (int t = 0; t < 6; ++t) {
        const float ov = t < 4 ? oa[t] : ob[t - 4];
#pragma unroll
        for (int k = 0; k < 4; ++k) { yt[2 * k] = fmaf(ov, bf_lo(wot[t][k]), yt[2 * k]); yt[2 * k + 1] = fmaf(ov, bf_hi(wot[t][k]), yt[2 * k + 1]); }
      }
      float r4[4];
#pragma unroll
      for (int k = 0; k < 4; ++k) { float t = tr32(yt[k], yt[k + 4]); t = tr16(t, t); r4[k] = t + dpp<XOR8>(t); }
      st_sc1(wsr, (wr16 && real) ? (P2PM ? WS_P2 + (half * 128 + grp) * 1280 + (j * 64 + col4) * 4 : WS_P2 + (half * 80 + j * 16 + (col4 >> 2)) * 2048 + grp * 16) : OOB, u32x4{san(r4[0]), san(r4[1]), san(r4[2]), san(r4[3])});
      STAMP(6);
      if (j == 1 && b < 160) {
        const unsigned o0 = real ? (P2PM ? WS_P2 + ((b / 80) * 128 + lane) * 1280 + (b % 80) * 16 : WS_P2 + b * 2048 + lane * 16) : OOB;
        u32x4 a0 = {0u, 0u, 0u, 0u}, a1 = {0u, 0u, 0u, 0u};
        bool k0 = false, k1 = false;
#pragma nounroll
        for (unsigned spin = 0; spin < GSPIN; ++spin) {
          asm volatile("" ::: "memory");
          const u32x4 n0 = ld_sc1(wsr, k0 ? OOB : o0), n1 = ld_sc1(wsr, k1 ? OOB : o0 + (P2PM ? 64 * 1280 : 1024));
          if (!k0) { a0 = n0; k0 = valid4(n0); }
          if (!k1) { a1 = n1; k1 = valid4(n1); }
          if (__all(k0 && k1)) break;
        }
        const float v0 = __uint_as_float(a0.x) + __uint_as_float(a1.x), v1 = __uint_as_float(a0.y) + __uint_as_float(a1.y);
        const float v2 = __uint_as_float(a0.z) + __uint_as_float(a1.z), v3 = __uint_as_float(a0.w) + __uint_as_float(a1.w);
        float t = tr16(tr32(v0, v2), tr32(v1, v3));
        t += dpp<XOR8>(t); t = sum_lo3(t);
        const __bf16 ov = (__bf16)t;
        __builtin_amdgcn_raw_buffer_store_b16(__builtin_bit_cast(unsigned short, ov), mkrsrc(out, D * 2), ((lane & 15) == 0 && real) ? (b * 4 + (lane >> 4)) * 2 : OOB, 0, 0);
        STAMP(7);
      }
    }
  }
  STAMP_FLUSH();
}

extern "C" void kernel_launch(void* const* d_in, const int* in_sizes, int n_in, void* d_out, int out_size, void* d_ws, size_t ws_size,
                              hipStream_t stream) {
  fused_kernel<<<256, NTHR, 0, stream>>>((const uint16_t*)d_in[0], (const uint16_t*)d_in[1], (const uint16_t*)d_in[2], (const uint16_t*)d_in[3],
                                         (const uint16_t*)d_in[4], (const uint16_t*)d_in[6], (const uint16_t*)d_in[7], (const uint16_t*)d_in[8],
                                         (const uint16_t*)d_in[9], (unsigned char*)d_ws, (uint16_t*)d_out);
}
```

```cpp
#include <hip/hip_runtime.h>
#include <hip/hip_bf16.h>
#include <stdint.h>
#include <stddef.h>

typedef unsigned u32x4 __attribute__((ext_vector_type(4)));
typedef unsigned u32x2 __attribute__((ext_vector_type(2)));
typedef float f32x4 __attribute__((ext_vector_type(4)));
typedef float f32x2 __attribute__((ext_vector_type(2)));
typedef __bf16 bf16x2 __attribute__((ext_vector_type(2)));
typedef __amdgpu_buffer_rsrc_t rsrc_t;

#define RING 4
#define AHEAD 1
#define INF 2
#define PMAX 6
#define NDYNU 192
#define XCHK 24
#define RATE 0.05f
#define FFN_T 2000

constexpr int D = 640, H = 32, SK = 8192, DH = 192, F = 1536;
constexpr int NHELP = 5, NSTR = 4, NWAVES = NHELP + NSTR, NTHR = NWAVES * 64;
constexpr int AUX_NT = 2, AUX_SC1 = 16;
constexpr unsigned OOB = 0x7ffffff0u;
constexpr unsigned WS_QO = 0;
constexpr unsigned WS_FO = WS_QO + 256 * 192 * 4;
constexpr unsigned WS_P1 = WS_FO + 16 * 1536 * 4;
constexpr unsigned WS_P2 = WS_P1 + 256 * 1024;
constexpr unsigned WS_TG = WS_P2 + 160 * 2048;
constexpr unsigned TGS = 128;
constexpr unsigned WS_END = WS_TG + 256 * TGS;
constexpr unsigned WS_STAMP = 2u << 20;
constexpr unsigned SENT = 0xFFFFFFFFu;
constexpr unsigned NWH = 8 * NSTR, NDYN = NDYNU, STAT = 1024 - NDYN, UPW = STAT / 8;
constexpr unsigned R0 = RING + PMAX + AHEAD;
constexpr unsigned NSW = UPW - R0 * NSTR;
static_assert(STAT % NWH == 0 && UPW >= R0 * NSTR && AHEAD < RING && INF <= RING && NDYN > 0 && XCHK <= NSW, "work split");

__device__ __forceinline__ float bf_lo(unsigned u) { return __builtin_bit_cast(float, u << 16); }
__device__ __forceinline__ float bf_hi(unsigned u) { return __builtin_bit_cast(float, u & 0xffff0000u); }
__device__ __forceinline__ float dot2(unsigned k, unsigned q, float acc) {
  return __builtin_amdgcn_fdot2_f32_bf16(__builtin_bit_cast(bf16x2, k), __builtin_bit_cast(bf16x2, q), acc, false);
}
template <int CTRL> __device__ __forceinline__ float dpp(float x) {
  return __builtin_bit_cast(float, __builtin_amdgcn_mov_dpp(__builtin_bit_cast(int, x), CTRL, 0xf, 0xf, true));
}
template <int CTRL> __device__ __forceinline__ unsigned udpp(unsigned x) { return (unsigned)__builtin_amdgcn_mov_dpp((int)x, CTRL, 0xf, 0xf, true); }
constexpr int XOR1 = 0xB1, XOR2 = 0x4E, HMIR = 0x141, XOR8 = 0x128, SHR1 = 0x111, SHR2 = 0x112, SHR4 = 0x114;
__device__ __forceinline__ float tr16(float a, float b) {
  auto s = __builtin_amdgcn_permlane16_swap(__float_as_uint(a), __float_as_uint(b), false, false);
  return __uint_as_float(s[0]) + __uint_as_float(s[1]);
}
__device__ __forceinline__ float tr32(float a, float b) {
  auto s = __builtin_amdgcn_permlane32_swap(__float_as_uint(a), __float_as_uint(b), false, false);
  return __uint_as_float(s[0]) + __uint_as_float(s[1]);
}
__device__ __forceinline__ float max16(float x) {
  auto s = __builtin_amdgcn_permlane16_swap(__float_as_uint(x), __float_as_uint(x), false, false);
  return fmaxf(__uint_as_float(s[0]), __uint_as_float(s[1]));
}
__device__ __forceinline__ float max32(float x) {
  auto s = __builtin_amdgcn_permlane32_swap(__float_as_uint(x), __float_as_uint(x), false, false);
  return fmaxf(__uint_as_float(s[0]), __uint_as_float(s[1]));
}
__device__ __forceinline__ float sum_hi3(float x) { x += dpp<XOR8>(x); x = tr16(x, x); return tr32(x, x); }
__device__ __forceinline__ float sum_lo3(float x) { x += dpp<XOR1>(x); x += dpp<XOR2>(x); x += dpp<HMIR>(x); return x; }
__device__ __forceinline__ float max_lo3(float x) { x = fmaxf(x, dpp<XOR1>(x)); x = fmaxf(x, dpp<XOR2>(x)); return fmaxf(x, dpp<HMIR>(x)); }
__device__ __forceinline__ unsigned san(float v) { unsigned u = __float_as_uint(v); return u < 0xFFFFFFFEu ? u : 0xFFFFFFFEu; }
__device__ __forceinline__ rsrc_t mkrsrc(const void* p, unsigned bytes) { return __builtin_amdgcn_make_buffer_rsrc((void*)p, 0, bytes, 0x00020000); }
__device__ __forceinline__ u32x4 ld_nt(rsrc_t r, unsigned voff) { return __builtin_amdgcn_raw_buffer_load_b128(r, voff, 0, AUX_NT); }
__device__ __forceinline__ u32x4 ld_sc1(rsrc_t r, unsigned voff) { return __builtin_amdgcn_raw_buffer_load_b128(r, voff, 0, AUX_SC1); }
__device__ __forceinline__ unsigned ld4_sc1(rsrc_t r, unsigned voff) { return __builtin_amdgcn_raw_buffer_load_b32(r, voff, 0, AUX_SC1); }
__device__ __forceinline__ void st_sc1(rsrc_t r, unsigned voff, u32x4 v) { __builtin_amdgcn_raw_buffer_store_b128(v, r, voff, 0, AUX_SC1); }
__device__ __forceinline__ void st4_sc1(rsrc_t r, unsigned voff, unsigned v) { __builtin_amdgcn_raw_buffer_store_b32(v, r, voff, 0, AUX_SC1); }
__device__ __forceinline__ bool valid4(u32x4 v) { return v.x != SENT && v.y != SENT && v.z != SENT && v.w != SENT; }
__device__ __forceinline__ unsigned lsb4(u32x4 v) { return (v.x & 1u) | ((v.y & 1u) << 1) | ((v.z & 1u) << 2) | ((v.w & 1u) << 3); }
__device__ __forceinline__ unsigned long long rt() { return __builtin_amdgcn_s_memrealtime(); }
__device__ __forceinline__ unsigned lds_ld(const unsigned* p) { return __hip_atomic_load(p, __ATOMIC_RELAXED, __HIP_MEMORY_SCOPE_WORKGROUP); }
__device__ __forceinline__ void lds_st(unsigned* p, unsigned v) { __hip_atomic_store(p, v, __ATOMIC_RELAXED, __HIP_MEMORY_SCOPE_WORKGROUP); }
__device__ __forceinline__ unsigned lds_add(unsigned* p) { return __hip_atomic_fetch_add(p, 1u, __ATOMIC_RELAXED, __HIP_MEMORY_SCOPE_WORKGROUP); }
__device__ __forceinline__ unsigned rfl(unsigned v) { return __builtin_amdgcn_readfirstlane(v); }
constexpr unsigned GSPIN = 1u << 15;
constexpr unsigned LSPIN = 1u << 19;
template <int SLEEP> __device__ __forceinline__ void lds_wait_ge(const unsigned* p, unsigned v) {
#pragma nounroll
  for (unsigned spin = 0; spin < LSPIN; ++spin) {
    if (rfl(lds_ld(p)) >= v) break;
    __builtin_amdgcn_s_sleep(SLEEP);
  }
  asm volatile("" ::: "memory");
}
#define WAIT_VM0() asm volatile("s_waitcnt vmcnt(0)" ::: "memory")
#define WAIT_LKM0() asm volatile("s_waitcnt lgkmcnt(0)" ::: "memory")
#define CFENCE() asm volatile("" ::: "memory")

#define STAMP(i) do {} while (0)
#define STAMP_FLUSH() do {} while (0)

template <int N> struct IC { static constexpr int value = N; };
template <int B, int E, class Fn> __device__ __forceinline__ void static_for(Fn&& f) { if constexpr (B < E) { f(IC<B>{}); static_for<B + 1, E>(f); } }

struct Lds {
  float mrg[NSTR][256];
  float ml[NSTR][2];
  unsigned qs[96];
  float os[64];
  float hs[16];
  unsigned qcnt;
  unsigned pre;
  unsigned qrdy;
  unsigned bdone;
  unsigned hrdy;
  unsigned oflag;
  unsigned tcons;
  unsigned tprod;
  unsigned sflag[4];
  unsigned tsn[8];
  u32x4 park[NSTR][PMAX][6][64];
};
static_assert(offsetof(Lds, sflag) % 16 == 0 && offsetof(Lds, os) % 16 == 0 && offsetof(Lds, qs) % 16 == 0 && offsetof(Lds, hs) % 16 == 0 && offsetof(Lds, park) % 16 == 0, "LDS alignment");
static_assert(sizeof(Lds) <= 160 * 1024, "LDS size");

struct GemvLoads { u32x4 w[10]; unsigned xs; };
__device__ __forceinline__ void gemv_issue(GemvLoads& g, rsrc_t xr, rsrc_t wr, unsigned ldB, unsigned row0, unsigned colB, int lane) {
  const unsigned off = (row0 + (lane >> 3)) * ldB + colB + (lane & 7) * 16;
#pragma unroll
  for (int i = 0; i < 10; ++i) g.w[i] = ld_nt(wr, off + i * 8 * ldB);
  g.xs = __builtin_amdgcn_raw_buffer_load_b32(xr, lane < 40 ? row0 * 2 + lane * 4 : OOB, 0, 0);
}
__device__ __forceinline__ void gemv_compute(const GemvLoads& g, float (&r)[4], int lane) {
  float acc[8];
#pragma unroll
  for (int k = 0; k < 8; ++k) acc[k] = 0.f;
  const int rg = lane >> 3;
#pragma unroll
  for (int i = 0; i < 10; ++i) {
    const unsigned xw = (unsigned)__builtin_amdgcn_ds_bpermute((i * 4 + (rg >> 1)) * 4, (int)g.xs);
    const float xv = (rg & 1) ? bf_hi(xw) : bf_lo(xw);
#pragma unroll
    for (int k = 0; k < 4; ++k) { acc[2 * k] = fmaf(xv, bf_lo(g.w[i][k]), acc[2 * k]); acc[2 * k + 1] = fmaf(xv, bf_hi(g.w[i][k]), acc[2 * k + 1]); }
  }
#pragma unroll
  for (int k = 0; k < 4; ++k) { float t = tr32(acc[k], acc[k + 4]); t = tr16(t, t); r[k] = t + dpp<XOR8>(t); }
}

__global__ void __launch_bounds__(NTHR) fused_kernel(const uint16_t* __restrict__ x, const uint16_t* __restrict__ kc, const uint16_t* __restrict__ vc,
                                                     const uint16_t* __restrict__ wq, const uint16_t* __restrict__ wo, const uint16_t* __restrict__ w1,
                                                     const uint16_t* __restrict__ w3, const uint16_t* __restrict__ w2, const uint16_t* __restrict__ gw,
                                                     unsigned char* __restrict__ ws, uint16_t* __restrict__ out) {
  __shared__ __attribute__((aligned(16))) Lds L;
  const int b = blockIdx.x, h = b >> 3, s = b & 7;
  const int tid = threadIdx.x, lane = tid & 63, wave = __builtin_amdgcn_readfirstlane(tid >> 6);
  const int rg = lane >> 3, c = lane & 7;
  STAMP(0);
  asm volatile("" :: "s"(x), "s"(kc), "s"(vc), "s"(wq), "s"(wo), "s"(w1), "s"(w3), "s"(w2), "s"(gw), "s"(ws), "s"(out));
  const rsrc_t wsr = mkrsrc(ws, WS_END);

  if (wave >= NHELP) {
    const int sw = wave - NHELP;
    const unsigned long long kb = (unsigned long long)(kc + (size_t)h * SK * DH), vb = (unsigned long long)(vc + (size_t)h * SK * DH);
    const u32x4 kr = {(unsigned)kb, (unsigned)(kb >> 32) & 0xffffu, (unsigned)(SK * DH * 2), 0x00020000u};
    const u32x4 vr = {(unsigned)vb, (unsigned)(vb >> 32) & 0xffffu, (unsigned)(SK * DH * 2), 0x00020000u};
    const unsigned loff = (unsigned)(rg * DH + c * 8) * 2;
    constexpr unsigned STEPB = 8 * DH * 2;
    u32x4 K[RING][3], V[RING][3];
#define RING_LOADS "s_nop 4\n\t" \
                   "buffer_load_dwordx4 %0, %6, %7, %9 offen nt\n\t" \
                   "buffer_load_dwordx4 %1, %6, %7, %9 offen offset:128 nt\n\t" \
                   "buffer_load_dwordx4 %2, %6, %7, %9 offen offset:256 nt\n\t" \
                   "buffer_load_dwordx4 %3, %6, %8, %9 offen nt\n\t" \
                   "buffer_load_dwordx4 %4, %6, %8, %9 offen offset:128 nt\n\t" \
                   "buffer_load_dwordx4 %5, %6, %8, %9 offen offset:256 nt"
    auto issue0 = [&](int slot, unsigned unit) {
      const unsigned so = unit * STEPB;
      asm volatile(RING_LOADS
                   : "=&v"(K[slot][0]), "=&v"(K[slot][1]), "=&v"(K[slot][2]), "=&v"(V[slot][0]), "=&v"(V[slot][1]), "=&v"(V[slot][2])
                   : "v"(loff), "s"(kr), "s"(vr), "s"(so));
    };
    auto issue = [&](int slot, unsigned unit) {
      const unsigned so = unit * STEPB;
      asm volatile(RING_LOADS
                   : "+v"(K[slot][0]), "+v"(K[slot][1]), "+v"(K[slot][2]), "+v"(V[slot][0]), "+v"(V[slot][1]), "+v"(V[slot][2])
                   : "v"(loff), "s"(kr), "s"(vr), "s"(so));
    };
#pragma unroll
    for (int i = 0; i < RING; ++i) {
      if (i >= INF) asm volatile("s_waitcnt vmcnt(%0)" :: "i"((INF - 1) * 6) : "memory");
      issue0(i, i * NWH + s * NSTR + sw);
      __builtin_amdgcn_sched_barrier(0);
      if (i == INF - 1) { __builtin_amdgcn_s_barrier(); __builtin_amdgcn_sched_barrier(0); }
    }
    unsigned pi = RING;
    auto private_unit = [&]() -> unsigned { return (pi++) * NWH + s * NSTR + sw; };
    int np = 0;
    {
      const unsigned pbase = rfl((unsigned)(size_t)&L.park[sw][0][0][0]);
#pragma nounroll
      while (np < PMAX) {
        asm volatile("s_waitcnt vmcnt(%0)" :: "i"((INF - 1) * 6) : "memory");
        if (rfl(lds_ld(&L.qcnt)) >= 3u) break;
        const unsigned so = private_unit() * STEPB, mb = pbase + np * 6144;
        unsigned keep;
        asm volatile("s_mov_b32 %0, m0\n\t"
                     "s_mov_b32 m0, %1\n\ts_nop 4\n\t"
                     "buffer_load_dwordx4 %2, %3, %5 offen nt lds\n\t"
                     "s_add_u32 m0, %1, 896\n\ts_nop 0\n\t"
                     "buffer_load_dwordx4 %2, %3, %5 offen offset:128 nt lds\n\t"
                     "s_add_u32 m0, %1, 1792\n\ts_nop 0\n\t"
                     "buffer_load_dwordx4 %2, %3, %5 offen offset:256 nt lds\n\t"
                     "s_add_u32 m0, %1, 3072\n\ts_nop 0\n\t"
                     "buffer_load_dwordx4 %2, %4, %5 offen nt lds\n\t"
                     "s_add_u32 m0, %1, 3968\n\ts_nop 0\n\t"
                     "buffer_load_dwordx4 %2, %4, %5 offen offset:128 nt lds\n\t"
                     "s_add_u32 m0, %1, 4864\n\ts_nop 0\n\t"
                     "buffer_load_dwordx4 %2, %4, %5 offen offset:256 nt lds\n\t"
                     "s_mov_b32 m0, %0"
                     : "=&s"(keep) : "s"(mb), "v"(loff), "s"(kr), "s"(vr), "s"(so) : "scc", "memory");
        ++np;
      }
    }
    STAMP(1);
    constexpr float C2 = 0.07216878364870322f * 1.4426950408889634f;
    float m = -INFINITY, l = 0.f, p_, alpha_;
    f32x2 acc2[12];
#pragma unroll
    for (int i = 0; i < 12; ++i) acc2[i] = f32x2{0.f, 0.f};
    u32x4 qp[3];
    auto scores = [&](const u32x4 (&Kx)[3]) {
      float d3[3];
#pragma unroll
      for (int jj = 0; jj < 3; ++jj) {
        d3[jj] = 0.f;
#pragma unroll
        for (int k = 0; k < 4; ++k) d3[jj] = dot2(Kx[jj][k], qp[jj][k], d3[jj]);
      }
      const float sc = sum_lo3(d3[0] + d3[1] + d3[2]) * C2;
      float pm = fmaxf(sc, dpp<XOR8>(sc)); pm = max32(max16(pm));
      const float mn = fmaxf(m, pm);
      alpha_ = __builtin_amdgcn_exp2f(m - mn); p_ = __builtin_amdgcn_exp2f(sc - mn);
      l = fmaf(l, alpha_, p_); m = mn;
    };
    auto accum = [&](const u32x4 (&Vx)[3]) {
      if (rfl(__float_as_uint(alpha_)) != 0x3f800000u) {
#pragma unroll
        for (int i = 0; i < 12; ++i) acc2[i] *= f32x2{alpha_, alpha_};
      }
#pragma unroll
      for (int jj = 0; jj < 3; ++jj)
#pragma unroll
        for (int k = 0; k < 4; ++k) acc2[jj * 4 + k] = __builtin_elementwise_fma(f32x2{p_, p_}, f32x2{bf_lo(Vx[jj][k]), bf_hi(Vx[jj][k])}, acc2[jj * 4 + k]);
#pragma unroll
      for (int jj = 0; jj < 3; ++jj) asm volatile("" : "+v"(acc2[jj * 4]), "+v"(acc2[jj * 4 + 1]), "+v"(acc2[jj * 4 + 2]), "+v"(acc2[jj * 4 + 3]));
    };
    auto consume = [&](int slot, auto younger) {
      constexpr int YN = decltype(younger)::value * 6;
      asm volatile("s_waitcnt vmcnt(%3)" : "+v"(K[slot][0]), "+v"(K[slot][1]), "+v"(K[slot][2]) : "i"(YN + 3));
      scores(K[slot]);
      asm volatile("s_waitcnt vmcnt(%3)" : "+v"(V[slot][0]), "+v"(V[slot][1]), "+v"(V[slot][2]) : "i"(YN));
      accum(V[slot]);
    };
    auto consume_parked = [&](int ip) {
      u32x4 T[3];
#pragma unroll
      for (int jj = 0; jj < 3; ++jj) T[jj] = L.park[sw][ip][jj][lane];
      scores(T);
      __builtin_amdgcn_sched_barrier(0);
#pragma unroll
      for (int jj = 0; jj < 3; ++jj) T[jj] = L.park[sw][ip][3 + jj][lane];
      accum(T);
    };
    lds_wait_ge<1>(&L.qcnt, 3u);
#pragma unroll
    for (int jj = 0; jj < 3; ++jj) qp[jj] = *(const u32x4*)&L.qs[jj * 32 + c * 4];
    STAMP(2);
    static_for<0, RING>([&](auto ic) {
      constexpr int i = decltype(ic)::value;
      consume(i, IC<RING - 1 - i + (i < AHEAD ? i : AHEAD)>{});
      if constexpr (i < AHEAD) issue(i, private_unit());
      __builtin_amdgcn_sched_barrier(0);
    });
    int rem = AHEAD;
    bool more = true;
#pragma nounroll
    do {
#pragma unroll
      for (int k = 0; k < RING; ++k) {
        if (rem > 0) {
          unsigned un = 1024u;
          if (pi < R0) un = private_unit();
          else if (more) {
            unsigned idx = 0;
            if (lane == 0) idx = lds_add(&L.tcons);
            idx = rfl(idx);
            if (idx < NSW) un = (R0 + idx / NSTR) * NWH + s * NSTR + idx % NSTR;
            else {
              idx -= NSW;
#pragma nounroll
              for (unsigned spin = 0; spin < LSPIN; ++spin) {
                const unsigned pr = rfl(lds_ld(&L.tprod));
                if (pr >> 31) { if (idx < (pr & 0xfffu)) un = STAT + ((pr >> 12) & 0xfffu) + idx; break; }
                __builtin_amdgcn_s_sleep(1);
              }
            }
          }
          more = un < 1024u;
          issue((k + AHEAD) % RING, un);
          rem += more ? 0 : -1;
          consume(k, IC<AHEAD>{});
          __builtin_amdgcn_sched_barrier(0);
        }
      }
      if (np > 0) { CFENCE(); --np; consume_parked(np); __builtin_amdgcn_sched_barrier(0); }
    } while (rem > 0 || np > 0);
#pragma unroll
    for (int r2 = 0; r2 < RING; ++r2)
      asm volatile("s_waitcnt vmcnt(0)" :: "v"(K[r2][0]), "v"(K[r2][1]), "v"(K[r2][2]), "v"(V[r2][0]), "v"(V[r2][1]), "v"(V[r2][2]));
    __builtin_amdgcn_sched_barrier(0);
    STAMP(3);
    float r6[6];
    {
      float r12[12];
#pragma unroll
      for (int i = 0; i < 12; ++i) r12[i] = tr32(acc2[i >> 1][i & 1], acc2[6 + (i >> 1)][i & 1]);
#pragma unroll
      for (int i = 0; i < 6; ++i) { const float t = tr16(r12[i], r12[i + 6]); r6[i] = t + dpp<XOR8>(t); }
    }
    const float lw = sum_hi3(l);
    if ((lane & 8) == 0) {
      const int slot = c * 4 + (lane >> 4);
      *(f32x4*)&L.mrg[sw][slot * 8] = f32x4{r6[0], r6[1], r6[2], r6[3]};
      *(f32x2*)&L.mrg[sw][slot * 8 + 4] = f32x2{r6[4], r6[5]};
    } else if (lane == 8) *(f32x2*)&L.ml[sw][0] = f32x2{m, lw};
    CFENCE();
    if (lane == 0) lds_st(&L.sflag[sw], 1u);
    STAMP(4);
    STAMP_FLUSH();
    return;
  }

  const unsigned long long tstart = rt();
  const rsrc_t xr = mkrsrc(x, D * 2);
  const int j = wave;
  const int rq = s >> 1, half = s & 1;
  const bool wr16 = (lane & 0x18) == 0;
  const unsigned col4 = c * 8 + (lane >> 5) * 4;
  const unsigned grp = h * 4 + rq;
  u32x4 wot[6], w2t[2];
  auto load_tiles = [&]() {
    const unsigned colB = (half * 320 + j * 64 + c * 8) * 2;
    const rsrc_t wor = mkrsrc(wo, H * DH * D * 2);
    const int cs = rq * 2 + (rg >> 2), i0 = (rg & 3) * 6;
#pragma unroll
    for (int t = 0; t < 6; ++t) { const int i = i0 + t; const unsigned d = (i >> 3) * 64 + cs * 8 + (i & 7); wot[t] = ld_nt(wor, (h * DH + d) * (D * 2) + colB); }
    const rsrc_t w2r = mkrsrc(w2, F * D * 2);
    w2t[0] = ld_nt(w2r, (grp * 12 + rg) * (D * 2) + colB);
    w2t[1] = ld_nt(w2r, rg < 4 ? (grp * 12 + 8 + rg) * (D * 2) + colB : OOB);
  };
  {
    GemvLoads g;
    float pa[4];
    if (j < 3) {
      const int p = lane >> 4, c4 = lane & 15;
      const unsigned o0 = WS_QO + ((h * 8 + p) * 192 + j * 64 + c4 * 4) * 4, o1 = o0 + 4 * 192 * 4;
      const unsigned om = wr16 ? WS_QO + (b * 192 + j * 64 + col4) * 4 : OOB;
      const u32x4 sn0 = ld_sc1(wsr, o0), sn1 = ld_sc1(wsr, o1), snm = ld_sc1(wsr, om);
      gemv_issue(g, xr, mkrsrc(wq, D * H * DH * 2), H * DH * 2, s * 80, (h * DH + j * 64) * 2, lane);
      __builtin_amdgcn_sched_barrier(0);
      if (tid < 12) (&L.qcnt)[tid] = 0u;
      WAIT_LKM0();
      __builtin_amdgcn_s_barrier();
      __builtin_amdgcn_sched_barrier(0);
      load_tiles();
      __builtin_amdgcn_sched_barrier(0);
      const unsigned t0 = lsb4(sn0), t1 = lsb4(sn1);
      asm volatile("" :: "v"(t0), "v"(t1), "v"(snm.x) : "memory");
      if (lane == 0) lds_add(&L.pre);
      gemv_compute(g, pa, lane);
      STAMP(1);
      asm volatile("" :: "v"(pa[0]), "v"(pa[1]), "v"(pa[2]), "v"(pa[3]) : "memory");
      lds_wait_ge<0>(&L.pre, 4u);
      st_sc1(wsr, om, u32x4{(__float_as_uint(pa[0]) & ~1u) | (~snm.x & 1u), (__float_as_uint(pa[1]) & ~1u) | (~snm.y & 1u),
                            (__float_as_uint(pa[2]) & ~1u) | (~snm.z & 1u), (__float_as_uint(pa[3]) & ~1u) | (~snm.w & 1u)});
      u32x4 a0 = {0u, 0u, 0u, 0u}, a1 = {0u, 0u, 0u, 0u};
      bool k0 = false, k1 = false;
      __builtin_amdgcn_s_sleep(8);
#pragma nounroll
      for (unsigned spin = 0; spin < GSPIN; ++spin) {
        const unsigned mask = lds_ld(&L.qrdy);
        WAIT_LKM0();
        const u32x4 n0 = ld_sc1(wsr, k0 ? OOB : o0), n1 = ld_sc1(wsr, k1 ? OOB : o1);
        WAIT_VM0();
        if (!k0) { a0 = n0; k0 = (lsb4(n0) ^ t0) == 15u || ((mask >> p) & 1u); }
        if (!k1) { a1 = n1; k1 = (lsb4(n1) ^ t1) == 15u || ((mask >> (p + 4)) & 1u); }
        if (__all(k0 && k1)) break;
        __builtin_amdgcn_s_sleep(2);
      }
      float q[4];
#pragma unroll
      for (int k = 0; k < 4; ++k) { float t = __uint_as_float(a0[k] & ~1u) + __uint_as_float(a1[k] & ~1u); t = tr16(t, t); q[k] = tr32(t, t); }
      if (p == 0) {
        bf16x2 lo = {(__bf16)q[0], (__bf16)q[1]}, hi = {(__bf16)q[2], (__bf16)q[3]};
        *(u32x2*)&L.qs[j * 32 + c4 * 2] = u32x2{__builtin_bit_cast(unsigned, lo), __builtin_bit_cast(unsigned, hi)};
      }
      CFENCE();
      if (lane == 0) lds_add(&L.qcnt);
      STAMP(2);
    } else {
      const int unit = j == 3 ? b : ((b & 1) ? -1 : 256 + (b >> 1));
      const unsigned u = unit < 0 ? 0 : unit, mat = u / 192, lb = (u % 192) >> 3, rp = u & 7;
      const unsigned tw = lane < 8 ? WS_TG + (h * 8 + lane) * TGS : (lane >= 32 && s == 0) ? WS_TG + (lane - 32) * 8 * TGS : OOB;
      unsigned snap = 0;
      if (j == 4) {
        const u32x2 snap2 = __builtin_amdgcn_raw_buffer_load_b64(wsr, tw, 0, AUX_SC1);
        snap = snap2.x;
        const u32x4 sv = {SENT, SENT, SENT, SENT};
        st_sc1(wsr, WS_P1 + b * 1024 + lane * 16, sv);
        st_sc1(wsr, b < 160 ? WS_P2 + b * 2048 + lane * 16 : OOB, sv);
        st_sc1(wsr, b < 160 ? WS_P2 + b * 2048 + 1024 + lane * 16 : OOB, sv);
        const unsigned u2 = lane < 16 ? b : 256 + (b >> 1);
        const bool on = lane < 16 || (lane < 32 && !(b & 1));
        st_sc1(wsr, on ? WS_FO + (((u2 / 192) * 8 + (u2 & 7)) * 1536 + ((u2 % 192) >> 3) * 64 + (lane & 15) * 4) * 4 : OOB, sv);
        __builtin_amdgcn_sched_barrier(0);
        __builtin_amdgcn_s_barrier();
        WAIT_VM0();
        if (lane < 8) L.tsn[lane] = snap2.y;
        CFENCE();
        if (lane == 0) lds_add(&L.pre);
      } else {
        __builtin_amdgcn_s_barrier();
      }
      gemv_issue(g, xr, mkrsrc(mat ? w3 : w1, unit < 0 ? 0 : D * F * 2), F * 2, rp * 80, lb * 128, lane);
      load_tiles();
      if (j == 4) {
        const unsigned long long tl = rt();
#pragma nounroll
        for (unsigned spin = 0; spin < LSPIN; ++spin) {
          if (rfl(lds_ld(&L.qcnt)) >= 3u) break;
          __builtin_amdgcn_s_sleep(2);
          if (rt() - tl > 800ull && (spin & 15u) == 0u) {
            const unsigned cur = ld4_sc1(wsr, lane < 8 ? tw : OOB);
            const unsigned qm = (unsigned)__ballot(lane < 8 && ((cur ^ snap) & 3u));
            if (lane == 0) lds_st(&L.qrdy, qm);
          }
        }
        CFENCE();
        st4_sc1(wsr, lane == s ? tw : OOB, snap ^ 1u);
        STAMP(1);
        const unsigned want = s == 0 ? 3u : 4u;
        bool seen = s == 0 ? lane < 32 : lane != 0;
#pragma nounroll
        for (unsigned spin = 0; spin < GSPIN; ++spin) {
          __builtin_amdgcn_s_sleep(16);
          const unsigned cur = ld4_sc1(wsr, seen ? OOB : tw);
          seen = seen || ((cur ^ snap) & want) != 0u;
          if (__all(seen)) break;
        }
        st4_sc1(wsr, (s == 0 && lane == 0) ? tw : OOB, snap ^ 7u);
        if (lane == 0) lds_st(&L.bdone, 1u);
        STAMP(2);
      }
      gemv_compute(g, pa, lane);
      st_sc1(wsr, (wr16 && unit >= 0) ? WS_FO + ((mat * 8 + rp) * 1536 + lb * 64 + col4) * 4 : OOB, u32x4{san(pa[0]), san(pa[1]), san(pa[2]), san(pa[3])});
    }
  }
  float gate;
  {
    const u32x4 gxa = __builtin_amdgcn_raw_buffer_load_b128(xr, lane * 20, 0, 0);
    const unsigned gxb = __builtin_amdgcn_raw_buffer_load_b32(xr, lane * 20 + 16, 0, 0);
    const rsrc_t gr = mkrsrc(gw, D * 2);
    const u32x4 gwa = __builtin_amdgcn_raw_buffer_load_b128(gr, lane * 20, 0, 0);
    const unsigned gwb = __builtin_amdgcn_raw_buffer_load_b32(gr, lane * 20 + 16, 0, 0);
    float z = 0.f;
#pragma unroll
    for (int k = 0; k < 4; ++k) { z = fmaf(bf_lo(gxa[k]), bf_lo(gwa[k]), z); z = fmaf(bf_hi(gxa[k]), bf_hi(gwa[k]), z); }
    z = fmaf(bf_lo(gxb), bf_lo(gwb), z); z = fmaf(bf_hi(gxb), bf_hi(gwb), z);
    z = sum_hi3(sum_lo3(z));
    gate = 1.f / (1.f + __expf(-z));
  }
  f32x2 wf[6][4];
#pragma unroll
  for (int t = 0; t < 6; ++t)
#pragma unroll
    for (int k = 0; k < 4; ++k) { wf[t][k] = f32x2{bf_lo(wot[t][k]), bf_hi(wot[t][k])}; asm volatile("" : "+v"(wf[t][k])); }
  if (j == 2) {
    lds_wait_ge<8>(&L.tcons, XCHK);
    const unsigned old = L.tsn[lane & 7];
    const unsigned ta = WS_TG + (h * 8 + (lane & 7)) * TGS + 4;
    st4_sc1(wsr, lane == s ? ta : OOB, ((unsigned)rt() << 1) | (~old & 1u));
    unsigned tv = 0;
    bool seen = lane >= 8;
#pragma nounroll
    for (unsigned spin = 0; spin < GSPIN; ++spin) {
      __builtin_amdgcn_s_sleep(16);
      const unsigned cur = ld4_sc1(wsr, seen ? OOB : ta);
      if (!seen) { tv = cur; seen = ((cur ^ old) & 1u) != 0u; }
      if (__all(seen)) break;
    }
    const int rel = (int)((tv & ~1u) - (rfl(tv) & ~1u)) >> 1;
    const float lag = (float)rel - sum_lo3((float)rel) * 0.125f;
    float n = fmaxf((float)(NDYN / 8) - lag * RATE, 0.f);
    n *= (float)NDYN / sum_lo3(n);
    unsigned cum = (unsigned)(n * 16.f);
    { unsigned t = udpp<SHR1>(cum); cum += (lane & 7) >= 1 ? t : 0u; t = udpp<SHR2>(cum); cum += (lane & 7) >= 2 ? t : 0u; t = udpp<SHR4>(cum); cum += (lane & 7) >= 4 ? t : 0u; }
    const unsigned hi = (lane & 7) == 7 ? NDYN : min((cum + 8u) >> 4, NDYN);
    const unsigned hprev = udpp<SHR1>(hi);
    const unsigned lo = (lane & 7) == 0 ? 0u : hprev;
    if (lane == s) lds_st(&L.tprod, 0x80000000u | (lo << 12) | (hi - lo));
  }
  if (j == 3) {
    const int part = lane >> 4, mr = lane & 15;
    const unsigned o = part < 3 ? WS_FO + (mr * 1536 + grp * 12 + part * 4) * 4 : OOB;
    lds_wait_ge<8>(&L.bdone, 1u);
#pragma nounroll
    while (rt() - tstart < FFN_T && !rfl(lds_ld(&L.sflag[0]))) __builtin_amdgcn_s_sleep(32);
    u32x4 a;
#pragma nounroll
    for (unsigned spin = 0; spin < GSPIN; ++spin) {
      CFENCE();
      a = ld_sc1(wsr, o);
      if (__all(valid4(a))) break;
      __builtin_amdgcn_s_sleep(16);
    }
    float hv[4];
#pragma unroll
    for (int k = 0; k < 4; ++k) {
      const float t = sum_lo3(__uint_as_float(a[k]));
      const float o8 = dpp<XOR8>(t);
      const float gv = (mr & 8) ? o8 : t, uv = (mr & 8) ? t : o8;
      hv[k] = gv / (1.f + __expf(-gv)) * uv;
    }
    if (mr == 0) *(f32x4*)&L.hs[part * 4] = f32x4{hv[0], hv[1], hv[2], hv[3]};
    CFENCE();
    if (lane == 0) lds_st(&L.hrdy, 1u);
  }
  lds_wait_ge<8>(&L.hrdy, 1u);
  f32x2 y2[4];
  {
    const float h0 = L.hs[rg] * (1.f - gate), h1 = L.hs[8 + rg] * (1.f - gate);
#pragma unroll
    for (int k = 0; k < 4; ++k) y2[k] = f32x2{h0 * bf_lo(w2t[0][k]) + h1 * bf_lo(w2t[1][k]), h0 * bf_hi(w2t[0][k]) + h1 * bf_hi(w2t[1][k])};
  }
  STAMP(3);
  if (j == 0) {
#pragma nounroll
    for (unsigned spin = 0; spin < LSPIN; ++spin) {
      const u32x4 f = *(volatile u32x4*)&L.sflag[0];
      if (rfl(f.x & f.y & f.z & f.w)) break;
      if (!rfl(f.x | f.y | f.z | f.w)) __builtin_amdgcn_s_sleep(2);
    }
    CFENCE();
    STAMP(4);
    {
      const int hf = lane & 1;
      f32x4 pv[NSTR]; f32x2 pm[NSTR];
#pragma unroll
      for (int q2 = 0; q2 < NSTR; ++q2) { pv[q2] = *(const f32x4*)&L.mrg[q2][lane * 4]; pm[q2] = *(const f32x2*)&L.ml[q2][0]; }
      float MM = pm[0].x;
#pragma unroll
      for (int q2 = 1; q2 < NSTR; ++q2) MM = fmaxf(MM, pm[q2].x);
      float o0 = 0.f, o1 = 0.f, o2 = 0.f, o3 = 0.f;
#pragma unroll
      for (int q2 = 0; q2 < NSTR; ++q2) {
        const float ww = __builtin_amdgcn_exp2f(pm[q2].x - MM);
        o0 = fmaf(ww, pv[q2].x, o0); o1 = fmaf(ww, pv[q2].y, o1);
        o2 = fmaf(ww, pv[q2].z, o2); o3 = fmaf(ww, hf ? pm[q2].y : pv[q2].w, o3);
      }
      if (hf) o2 = MM;
      st_sc1(wsr, WS_P1 + b * 1024 + lane * 16, u32x4{san(o0), san(o1), san(o2), san(o3)});
    }
    STAMP(7);
    const unsigned o = WS_P1 + (h * 8 + c) * 1024 + (rq * 8 + rg) * 32;
    u32x4 A = {0u, 0u, 0u, 0u}, B = {0u, 0u, 0u, 0u};
    bool ok = false;
#pragma nounroll
    for (unsigned spin = 0; spin < GSPIN; ++spin) {
      CFENCE();
      const u32x4 nA = ld_sc1(wsr, ok ? OOB : o), nB = ld_sc1(wsr, ok ? OOB : o + 16);
      if (!ok) { A = nA; B = nB; ok = valid4(nA) && valid4(nB); }
      if (__all(ok)) break;
    }
    STAMP(5);
    const float mp = __uint_as_float(B.z);
    const float M = max_lo3(mp);
    const float w = __builtin_amdgcn_exp2f(mp - M);
    const float sc = gate * __builtin_amdgcn_rcpf(sum_lo3(w * __uint_as_float(B.w)));
    const float o0 = sum_lo3(w * __uint_as_float(A.x)) * sc, o1 = sum_lo3(w * __uint_as_float(A.y)) * sc, o2 = sum_lo3(w * __uint_as_float(A.z)) * sc;
    const float o3 = sum_lo3(w * __uint_as_float(A.w)) * sc, o4 = sum_lo3(w * __uint_as_float(B.x)) * sc, o5 = sum_lo3(w * __uint_as_float(B.y)) * sc;
    if (c == 0) { *(f32x4*)&L.os[rg * 8] = f32x4{o0, o1, o2, o3}; *(f32x2*)&L.os[rg * 8 + 4] = f32x2{o4, o5}; }
    CFENCE();
    if (lane == 0) lds_st(&L.oflag, 1u);
    CFENCE();
  } else {
    lds_wait_ge<4>(&L.sflag[0], 1u);
    lds_wait_ge<0>(&L.oflag, 1u);
  }
  {
    const f32x4 oa = *(const f32x4*)&L.os[rg * 8];
    const f32x2 ob = *(const f32x2*)&L.os[rg * 8 + 4];
#pragma unroll
    for (int t = 0; t < 6; ++t) {
      const float ov = t < 4 ? oa[t] : ob[t - 4];
#pragma unroll
      for (int k = 0; k < 4; ++k) y2[k] = __builtin_elementwise_fma(f32x2{ov, ov}, wf[t][k], y2[k]);
    }
    float r4[4];
#pragma unroll
    for (int k = 0; k < 4; ++k) { float t = tr32(y2[k >> 1][k & 1], y2[2 + (k >> 1)][k & 1]); t = tr16(t, t); r4[k] = t + dpp<XOR8>(t); }
    st_sc1(wsr, wr16 ? WS_P2 + (half * 80 + j * 16 + (col4 >> 2)) * 2048 + grp * 16 : OOB, u32x4{san(r4[0]), san(r4[1]), san(r4[2]), san(r4[3])});
  }
  STAMP(6);
  if (j == 1 && b < 160) {
    const unsigned o0 = WS_P2 + b * 2048 + lane * 16;
    u32x4 a0 = {0u, 0u, 0u, 0u}, a1 = {0u, 0u, 0u, 0u};
    bool k0 = false, k1 = false;
#pragma nounroll
    for (unsigned spin = 0; spin < GSPIN; ++spin) {
      CFENCE();
      const u32x4 n0 = ld_sc1(wsr, k0 ? OOB : o0), n1 = ld_sc1(wsr, k1 ? OOB : o0 + 1024);
      if (!k0) { a0 = n0; k0 = valid4(n0); }
      if (!k1) { a1 = n1; k1 = valid4(n1); }
      if (__all(k0 && k1)) break;
    }
    const float v0 = __uint_as_float(a0.x) + __uint_as_float(a1.x), v1 = __uint_as_float(a0.y) + __uint_as_float(a1.y);
    const float v2 = __uint_as_float(a0.z) + __uint_as_float(a1.z), v3 = __uint_as_float(a0.w) + __uint_as_float(a1.w);
    float t = tr16(tr32(v0, v2), tr32(v1, v3));
    t += dpp<XOR8>(t); t = sum_lo3(t);
    const __bf16 ov = (__bf16)t;
    __builtin_amdgcn_raw_buffer_store_b16(__builtin_bit_cast(unsigned short, ov), mkrsrc(out, D * 2), (lane & 15) == 0 ? (b * 4 + (lane >> 4)) * 2 : OOB, 0, 0);
    STAMP(7);
  }
  STAMP_FLUSH();
}

extern "C" void kernel_launch(void* const* d_in, const int* in_sizes, int n_in, void* d_out, int out_size, void* d_ws, size_t ws_size,
                              hipStream_t stream) {
  fused_kernel<<<256, NTHR, 0, stream>>>((const uint16_t*)d_in[0], (const uint16_t*)d_in[1], (const uint16_t*)d_in[2], (const uint16_t*)d_in[3],
                                         (const uint16_t*)d_in[4], (const uint16_t*)d_in[6], (const uint16_t*)d_in[7], (const uint16_t*)d_in[8],
                                         (const uint16_t*)d_in[9], (unsigned char*)d_ws, (uint16_t*)d_out);
}
```

```cpp
#include <hip/hip_runtime.h>
#include <hip/hip_bf16.h>
#include <stdint.h>
#include <stddef.h>

typedef unsigned u32x4 __attribute__((ext_vector_type(4)));
typedef unsigned u32x2 __attribute__((ext_vector_type(2)));
typedef float f32x4 __attribute__((ext_vector_type(4)));
typedef float f32x2 __attribute__((ext_vector_type(2)));
typedef __bf16 bf16x2 __attribute__((ext_vector_type(2)));
typedef __amdgpu_buffer_rsrc_t rsrc_t;

#define RING 4
#define AHEAD 1
#define INF 1
#define PMAX 6
#define NDYNU 128
#define XCHK 28
#define RATE 0.05f
#define TLX 1000
#define NDYN2U 64
#define R2 16
#define FFN_T 2000

constexpr int D = 640, H = 32, SK = 8192, DH = 192, F = 1536;
constexpr int NHELP = 5, NSTR = 4, NWAVES = NHELP + NSTR, NTHR = NWAVES * 64;
constexpr int AUX_NT = 2, AUX_SC1 = 16;
constexpr unsigned OOB = 0x7ffffff0u;
constexpr unsigned WS_QO = 0;
constexpr unsigned WS_FO = WS_QO + 256 * 192 * 4;
constexpr unsigned WS_P1 = WS_FO + 16 * 1536 * 4;
constexpr unsigned WS_P2 = WS_P1 + 256 * 1024;
constexpr unsigned WS_TG = WS_P2 + 160 * 2048;
constexpr unsigned TGS = 128;
constexpr unsigned WS_END = WS_TG + 256 * TGS;
constexpr unsigned WS_STAMP = 2u << 20;
constexpr unsigned SENT = 0xFFFFFFFFu;
constexpr unsigned NWH = 8 * NSTR, NDYN = NDYNU, NDYN2 = NDYN2U, STAT = 1024 - NDYN - NDYN2, UPW = STAT / 8;
constexpr unsigned R0 = RING + PMAX + AHEAD;
constexpr unsigned NSW = UPW - R0 * NSTR;
static_assert(STAT % NWH == 0 && UPW >= R0 * NSTR && AHEAD < RING && INF <= RING && NDYN > 0 && XCHK <= NSW && R2 <= NSW - XCHK, "work split");

__device__ __forceinline__ float bf_lo(unsigned u) { return __builtin_bit_cast(float, u << 16); }
__device__ __forceinline__ float bf_hi(unsigned u) { return __builtin_bit_cast(float, u & 0xffff0000u); }
__device__ __forceinline__ float dot2(unsigned k, unsigned q, float acc) {
  return __builtin_amdgcn_fdot2_f32_bf16(__builtin_bit_cast(bf16x2, k), __builtin_bit_cast(bf16x2, q), acc, false);
}
template <int CTRL> __device__ __forceinline__ float dpp(float x) {
  return __builtin_bit_cast(float, __builtin_amdgcn_mov_dpp(__builtin_bit_cast(int, x), CTRL, 0xf, 0xf, true));
}
template <int CTRL> __device__ __forceinline__ unsigned udpp(unsigned x) { return (unsigned)__builtin_amdgcn_mov_dpp((int)x, CTRL, 0xf, 0xf, true); }
constexpr int XOR1 = 0xB1, XOR2 = 0x4E, HMIR = 0x141, XOR8 = 0x128, SHR1 = 0x111, SHR2 = 0x112, SHR4 = 0x114;
__device__ __forceinline__ float tr16(float a, float b) {
  auto s = __builtin_amdgcn_permlane16_swap(__float_as_uint(a), __float_as_uint(b), false, false);
  return __uint_as_float(s[0]) + __uint_as_float(s[1]);
}
__device__ __forceinline__ float tr32(float a, float b) {
  auto s = __builtin_amdgcn_permlane32_swap(__float_as_uint(a), __float_as_uint(b), false, false);
  return __uint_as_float(s[0]) + __uint_as_float(s[1]);
}
__device__ __forceinline__ float max16(float x) {
  auto s = __builtin_amdgcn_permlane16_swap(__float_as_uint(x), __float_as_uint(x), false, false);
  return fmaxf(__uint_as_float(s[0]), __uint_as_float(s[1]));
}
__device__ __forceinline__ float max32(float x) {
  auto s = __builtin_amdgcn_permlane32_swap(__float_as_uint(x), __float_as_uint(x), false, false);
  return fmaxf(__uint_as_float(s[0]), __uint_as_float(s[1]));
}
__device__ __forceinline__ float sum_hi3(float x) { x += dpp<XOR8>(x); x = tr16(x, x); return tr32(x, x); }
__device__ __forceinline__ float sum_lo3(float x) { x += dpp<XOR1>(x); x += dpp<XOR2>(x); x += dpp<HMIR>(x); return x; }
__device__ __forceinline__ float max_lo3(float x) { x = fmaxf(x, dpp<XOR1>(x)); x = fmaxf(x, dpp<XOR2>(x)); return fmaxf(x, dpp<HMIR>(x)); }
__device__ __forceinline__ unsigned san(float v) { unsigned u = __float_as_uint(v); return u < 0xFFFFFFFEu ? u : 0xFFFFFFFEu; }
__device__ __forceinline__ rsrc_t mkrsrc(const void* p, unsigned bytes) { return __builtin_amdgcn_make_buffer_rsrc((void*)p, 0, bytes, 0x00020000); }
__device__ __forceinline__ u32x4 ld_nt(rsrc_t r, unsigned voff) { return __builtin_amdgcn_raw_buffer_load_b128(r, voff, 0, AUX_NT); }
__device__ __forceinline__ u32x4 ld_sc1(rsrc_t r, unsigned voff) { return __builtin_amdgcn_raw_buffer_load_b128(r, voff, 0, AUX_SC1); }
__device__ __forceinline__ unsigned ld4_sc1(rsrc_t r, unsigned voff) { return __builtin_amdgcn_raw_buffer_load_b32(r, voff, 0, AUX_SC1); }
__device__ __forceinline__ void st_sc1(rsrc_t r, unsigned voff, u32x4 v) { __builtin_amdgcn_raw_buffer_store_b128(v, r, voff, 0, AUX_SC1); }
__device__ __forceinline__ void st4_sc1(rsrc_t r, unsigned voff, unsigned v) { __builtin_amdgcn_raw_buffer_store_b32(v, r, voff, 0, AUX_SC1); }
__device__ __forceinline__ bool valid4(u32x4 v) { return v.x != SENT && v.y != SENT && v.z != SENT && v.w != SENT; }
__device__ __forceinline__ unsigned lsb4(u32x4 v) { return (v.x & 1u) | ((v.y & 1u) << 1) | ((v.z & 1u) << 2) | ((v.w & 1u) << 3); }
__device__ __forceinline__ unsigned long long rt() { return __builtin_amdgcn_s_memrealtime(); }
__device__ __forceinline__ unsigned lds_ld(const unsigned* p) { return __hip_atomic_load(p, __ATOMIC_RELAXED, __HIP_MEMORY_SCOPE_WORKGROUP); }
__device__ __forceinline__ void lds_st(unsigned* p, unsigned v) { __hip_atomic_store(p, v, __ATOMIC_RELAXED, __HIP_MEMORY_SCOPE_WORKGROUP); }
__device__ __forceinline__ unsigned lds_add(unsigned* p) { return __hip_atomic_fetch_add(p, 1u, __ATOMIC_RELAXED, __HIP_MEMORY_SCOPE_WORKGROUP); }
__device__ __forceinline__ unsigned rfl(unsigned v) { return __builtin_amdgcn_readfirstlane(v); }
constexpr unsigned GSPIN = 1u << 15;
constexpr unsigned LSPIN = 1u << 19;
template <int SLEEP> __device__ __forceinline__ void lds_wait_ge(const unsigned* p, unsigned v) {
#pragma nounroll
  for (unsigned spin = 0; spin < LSPIN; ++spin) {
    if (rfl(lds_ld(p)) >= v) break;
    __builtin_amdgcn_s_sleep(SLEEP);
  }
  asm volatile("" ::: "memory");
}
#define WAIT_VM0() asm volatile("s_waitcnt vmcnt(0)" ::: "memory")
#define WAIT_LKM0() asm volatile("s_waitcnt lgkmcnt(0)" ::: "memory")
#define CFENCE() asm volatile("" ::: "memory")

#define STAMP(i) do {} while (0)
#define STAMP_FLUSH() do {} while (0)

template <int N> struct IC { static constexpr int value = N; };
template <int B, int E, class Fn> __device__ __forceinline__ void static_for(Fn&& f) { if constexpr (B < E) { f(IC<B>{}); static_for<B + 1, E>(f); } }

struct Lds {
  float mrg[NSTR][256];
  float ml[NSTR][2];
  unsigned qs[96];
  float os[64];
  float hs[16];
  unsigned qcnt;
  unsigned pre;
  unsigned qrdy;
  unsigned bdone;
  unsigned hrdy;
  unsigned oflag;
  unsigned tcons;
  unsigned tprod;
  unsigned sflag[4];
  unsigned tsn[2][8];
  unsigned tprod2, pad2[3];
  u32x4 park[NSTR][PMAX][6][64];
};
static_assert(offsetof(Lds, sflag) % 16 == 0 && offsetof(Lds, os) % 16 == 0 && offsetof(Lds, qs) % 16 == 0 && offsetof(Lds, hs) % 16 == 0 && offsetof(Lds, park) % 16 == 0, "LDS alignment");
static_assert(sizeof(Lds) <= 160 * 1024, "LDS size");

struct GemvLoads { u32x4 w[10]; unsigned xs; };
__device__ __forceinline__ void gemv_issue(GemvLoads& g, rsrc_t xr, rsrc_t wr, unsigned ldB, unsigned row0, unsigned colB, int lane) {
  const unsigned off = (row0 + (lane >> 3)) * ldB + colB + (lane & 7) * 16;
#pragma unroll
  for (int i = 0; i < 10; ++i) g.w[i] = ld_nt(wr, off + i * 8 * ldB);
  g.xs = __builtin_amdgcn_raw_buffer_load_b32(xr, lane < 40 ? row0 * 2 + lane * 4 : OOB, 0, 0);
}
__device__ __forceinline__ void gemv_compute(const GemvLoads& g, float (&r)[4], int lane) {
  float acc[8];
#pragma unroll
  for (int k = 0; k < 8; ++k) acc[k] = 0.f;
  const int rg = lane >> 3;
#pragma unroll
  for (int i = 0; i < 10; ++i) {
    const unsigned xw = (unsigned)__builtin_amdgcn_ds_bpermute((i * 4 + (rg >> 1)) * 4, (int)g.xs);
    const float xv = (rg & 1) ? bf_hi(xw) : bf_lo(xw);
#pragma unroll
    for (int k = 0; k < 4; ++k) { acc[2 * k] = fmaf(xv, bf_lo(g.w[i][k]), acc[2 * k]); acc[2 * k + 1] = fmaf(xv, bf_hi(g.w[i][k]), acc[2 * k + 1]); }
  }
#pragma unroll
  for (int k = 0; k < 4; ++k) { float t = tr32(acc[k], acc[k + 4]); t = tr16(t, t); r[k] = t + dpp<XOR8>(t); }
}

__global__ void __launch_bounds__(NTHR) fused_kernel(const uint16_t* __restrict__ x, const uint16_t* __restrict__ kc, const uint16_t* __restrict__ vc,
                                                     const uint16_t* __restrict__ wq, const uint16_t* __restrict__ wo, const uint16_t* __restrict__ w1,
                                                     const uint16_t* __restrict__ w3, const uint16_t* __restrict__ w2, const uint16_t* __restrict__ gw,
                                                     unsigned char* __restrict__ ws, uint16_t* __restrict__ out) {
  __shared__ __attribute__((aligned(16))) Lds L;
  const int b = blockIdx.x, h = b >> 3, s = b & 7;
  const int tid = threadIdx.x, lane = tid & 63, wave = __builtin_amdgcn_readfirstlane(tid >> 6);
  const int rg = lane >> 3, c = lane & 7;
  STAMP(0);
  asm volatile("" :: "s"(x), "s"(kc), "s"(vc), "s"(wq), "s"(wo), "s"(w1), "s"(w3), "s"(w2), "s"(gw), "s"(ws), "s"(out));
  const rsrc_t wsr = mkrsrc(ws, WS_END);

  if (wave >= NHELP) {
    const int sw = wave - NHELP;
    const unsigned long long kb = (unsigned long long)(kc + (size_t)h * SK * DH), vb = (unsigned long long)(vc + (size_t)h * SK * DH);
    const u32x4 kr = {(unsigned)kb, (unsigned)(kb >> 32) & 0xffffu, (unsigned)(SK * DH * 2), 0x00020000u};
    const u32x4 vr = {(unsigned)vb, (unsigned)(vb >> 32) & 0xffffu, (unsigned)(SK * DH * 2), 0x00020000u};
    const unsigned loff = (unsigned)(rg * DH + c * 8) * 2;
    constexpr unsigned STEPB = 8 * DH * 2;
    u32x4 K[RING][3], V[RING][3];
#define RING_LOADS "s_nop 4\n\t" \
                   "buffer_load_dwordx4 %0, %6, %7, %9 offen nt\n\t" \
                   "buffer_load_dwordx4 %1, %6, %7, %9 offen offset:128 nt\n\t" \
                   "buffer_load_dwordx4 %2, %6, %7, %9 offen offset:256 nt\n\t" \
                   "buffer_load_dwordx4 %3, %6, %8, %9 offen nt\n\t" \
                   "buffer_load_dwordx4 %4, %6, %8, %9 offen offset:128 nt\n\t" \
                   "buffer_load_dwordx4 %5, %6, %8, %9 offen offset:256 nt"
    auto issue0 = [&](int slot, unsigned unit) {
      const unsigned so = unit * STEPB;
      asm volatile(RING_LOADS
                   : "=&v"(K[slot][0]), "=&v"(K[slot][1]), "=&v"(K[slot][2]), "=&v"(V[slot][0]), "=&v"(V[slot][1]), "=&v"(V[slot][2])
                   : "v"(loff), "s"(kr), "s"(vr), "s"(so));
    };
    auto issue = [&](int slot, unsigned unit) {
      const unsigned so = unit * STEPB;
      asm volatile(RING_LOADS
                   : "+v"(K[slot][0]), "+v"(K[slot][1]), "+v"(K[slot][2]), "+v"(V[slot][0]), "+v"(V[slot][1]), "+v"(V[slot][2])
                   : "v"(loff), "s"(kr), "s"(vr), "s"(so));
    };
#pragma unroll
    for (int i = 0; i < RING; ++i) {
      if (i >= INF) asm volatile("s_waitcnt vmcnt(%0)" :: "i"((INF - 1) * 6) : "memory");
      issue0(i, i * NWH + s * NSTR + sw);
      __builtin_amdgcn_sched_barrier(0);
      if (i == INF - 1) { __builtin_amdgcn_s_barrier(); __builtin_amdgcn_sched_barrier(0); }
    }
    unsigned pi = RING;
    auto private_unit = [&]() -> unsigned { return (pi++) * NWH + s * NSTR + sw; };
    int np = 0;
    {
      const unsigned pbase = rfl((unsigned)(size_t)&L.park[sw][0][0][0]);
#pragma nounroll
      while (np < PMAX) {
        asm volatile("s_waitcnt vmcnt(%0)" :: "i"((INF - 1) * 6) : "memory");
        if (rfl(lds_ld(&L.qcnt)) >= 3u) break;
        const unsigned so = private_unit() * STEPB, mb = pbase + np * 6144;
        unsigned keep;
        asm volatile("s_mov_b32 %0, m0\n\t"
                     "s_mov_b32 m0, %1\n\ts_nop 4\n\t"
                     "buffer_load_dwordx4 %2, %3, %5 offen nt lds\n\t"
                     "s_add_u32 m0, %1, 896\n\ts_nop 0\n\t"
                     "buffer_load_dwordx4 %2, %3, %5 offen offset:128 nt lds\n\t"
                     "s_add_u32 m0, %1, 1792\n\ts_nop 0\n\t"
                     "buffer_load_dwordx4 %2, %3, %5 offen offset:256 nt lds\n\t"
                     "s_add_u32 m0, %1, 3072\n\ts_nop 0\n\t"
                     "buffer_load_dwordx4 %2, %4, %5 offen nt lds\n\t"
                     "s_add_u32 m0, %1, 3968\n\ts_nop 0\n\t"
                     "buffer_load_dwordx4 %2, %4, %5 offen offset:128 nt lds\n\t"
                     "s_add_u32 m0, %1, 4864\n\ts_nop 0\n\t"
                     "buffer_load_dwordx4 %2, %4, %5 offen offset:256 nt lds\n\t"
                     "s_mov_b32 m0, %0"
                     : "=&s"(keep) : "s"(mb), "v"(loff), "s"(kr), "s"(vr), "s"(so) : "scc", "memory");
        ++np;
      }
    }
    STAMP(1);
    constexpr float C2 = 0.07216878364870322f * 1.4426950408889634f;
    float m = -INFINITY, l = 0.f, p_, alpha_;
    f32x2 acc2[12];
#pragma unroll
    for (int i = 0; i < 12; ++i) acc2[i] = f32x2{0.f, 0.f};
    u32x4 qp[3];
    auto scores = [&](const u32x4 (&Kx)[3]) {
      float d3[3];
#pragma unroll
      for (int jj = 0; jj < 3; ++jj) {
        d3[jj] = 0.f;
#pragma unroll
        for (int k = 0; k < 4; ++k) d3[jj] = dot2(Kx[jj][k], qp[jj][k], d3[jj]);
      }
      const float sc = sum_lo3(d3[0] + d3[1] + d3[2]) * C2;
      float pm = fmaxf(sc, dpp<XOR8>(sc)); pm = max32(max16(pm));
      const float mn = fmaxf(m, pm);
      alpha_ = __builtin_amdgcn_exp2f(m - mn); p_ = __builtin_amdgcn_exp2f(sc - mn);
      l = fmaf(l, alpha_, p_); m = mn;
    };
    auto accum = [&](const u32x4 (&Vx)[3]) {
      if (rfl(__float_as_uint(alpha_)) != 0x3f800000u) {
#pragma unroll
        for (int i = 0; i < 12; ++i) acc2[i] *= f32x2{alpha_, alpha_};
      }
#pragma unroll
      for (int jj = 0; jj < 3; ++jj)
#pragma unroll
        for (int k = 0; k < 4; ++k) acc2[jj * 4 + k] = __builtin_elementwise_fma(f32x2{p_, p_}, f32x2{bf_lo(Vx[jj][k]), bf_hi(Vx[jj][k])}, acc2[jj * 4 + k]);
#pragma unroll
      for (int jj = 0; jj < 3; ++jj) asm volatile("" : "+v"(acc2[jj * 4]), "+v"(acc2[jj * 4 + 1]), "+v"(acc2[jj * 4 + 2]), "+v"(acc2[jj * 4 + 3]));
    };
    auto consume = [&](int slot, auto younger) {
      constexpr int YN = decltype(younger)::value * 6;
      asm volatile("s_waitcnt vmcnt(%3)" : "+v"(K[slot][0]), "+v"(K[slot][1]), "+v"(K[slot][2]) : "i"(YN + 3));
      scores(K[slot]);
      asm volatile("s_waitcnt vmcnt(%3)" : "+v"(V[slot][0]), "+v"(V[slot][1]), "+v"(V[slot][2]) : "i"(YN));
      accum(V[slot]);
    };
    auto consume_parked = [&](int ip) {
      u32x4 T[3];
#pragma unroll
      for (int jj = 0; jj < 3; ++jj) T[jj] = L.park[sw][ip][jj][lane];
      scores(T);
      __builtin_amdgcn_sched_barrier(0);
#pragma unroll
      for (int jj = 0; jj < 3; ++jj) T[jj] = L.park[sw][ip][3 + jj][lane];
      accum(T);
    };
    lds_wait_ge<1>(&L.qcnt, 3u);
#pragma unroll
    for (int jj = 0; jj < 3; ++jj) qp[jj] = *(const u32x4*)&L.qs[jj * 32 + c * 4];
    STAMP(2);
    static_for<0, RING>([&](auto ic) {
      constexpr int i = decltype(ic)::value;
      consume(i, IC<RING - 1 - i + (i < AHEAD ? i : AHEAD)>{});
      if constexpr (i < AHEAD) issue(i, private_unit());
      __builtin_amdgcn_sched_barrier(0);
    });
    int rem = AHEAD;
    bool more = true;
#pragma nounroll
    do {
#pragma unroll
      for (int k = 0; k < RING; ++k) {
        if (rem > 0) {
          unsigned un = 1024u;
          if (pi < R0) un = private_unit();
          else if (more) {
            unsigned idx = 0;
            if (lane == 0) idx = lds_add(&L.tcons);
            idx = rfl(idx);
            if (idx < NSW) un = (R0 + idx / NSTR) * NWH + s * NSTR + idx % NSTR;
            else {
              idx -= NSW;
#pragma nounroll
              for (unsigned spin = 0; spin < LSPIN; ++spin) {
                const unsigned pr = rfl(lds_ld(&L.tprod));
                if (pr >> 31) {
                  if (idx < (pr & 0xfffu)) { un = STAT + ((pr >> 12) & 0xfffu) + idx; break; }
                  if (NDYN2 == 0) break;
                  const unsigned p2 = rfl(lds_ld(&L.tprod2)), i2 = idx - (pr & 0xfffu);
                  if (p2 >> 31) { if (i2 < (p2 & 0xfffu)) un = STAT + NDYN + ((p2 >> 12) & 0xfffu) + i2; break; }
                }
                __builtin_amdgcn_s_sleep(1);
              }
            }
          }
          more = un < 1024u;
          issue((k + AHEAD) % RING, un);
          rem += more ? 0 : -1;
          consume(k, IC<AHEAD>{});
          __builtin_amdgcn_sched_barrier(0);
        }
      }
      if (np > 0) { CFENCE(); --np; consume_parked(np); __builtin_amdgcn_sched_barrier(0); }
    } while (rem > 0 || np > 0);
#pragma unroll
    for (int r2 = 0; r2 < RING; ++r2)
      asm volatile("s_waitcnt vmcnt(0)" :: "v"(K[r2][0]), "v"(K[r2][1]), "v"(K[r2][2]), "v"(V[r2][0]), "v"(V[r2][1]), "v"(V[r2][2]));
    __builtin_amdgcn_sched_barrier(0);
    STAMP(3);
    float r6[6];
    {
      float r12[12];
#pragma unroll
      for (int i = 0; i < 12; ++i) r12[i] = tr32(acc2[i >> 1][i & 1], acc2[6 + (i >> 1)][i & 1]);
#pragma unroll
      for (int i = 0; i < 6; ++i) { const float t = tr16(r12[i], r12[i + 6]); r6[i] = t + dpp<XOR8>(t); }
    }
    const float lw = sum_hi3(l);
    if ((lane & 8) == 0) {
      const int slot = c * 4 + (lane >> 4);
      *(f32x4*)&L.mrg[sw][slot * 8] = f32x4{r6[0], r6[1], r6[2], r6[3]};
      *(f32x2*)&L.mrg[sw][slot * 8 + 4] = f32x2{r6[4], r6[5]};
    } else if (lane == 8) *(f32x2*)&L.ml[sw][0] = f32x2{m, lw};
    CFENCE();
    if (lane == 0) lds_st(&L.sflag[sw], 1u);
    STAMP(4);
    STAMP_FLUSH();
    return;
  }

  const unsigned long long tstart = rt();
  const rsrc_t xr = mkrsrc(x, D * 2);
  const int j = wave;
  const int rq = s >> 1, half = s & 1;
  const bool wr16 = (lane & 0x18) == 0;
  const unsigned col4 = c * 8 + (lane >> 5) * 4;
  const unsigned grp = h * 4 + rq;
  u32x4 wot[6], w2t[2];
#pragma unroll
  for (int t = 0; t < 6; ++t) wot[t] = u32x4{0u, 0u, 0u, 0u};
  w2t[0] = w2t[1] = u32x4{0u, 0u, 0u, 0u};
  auto load_tiles = [&]() {
    const unsigned colB = (half * 320 + j * 64 + c * 8) * 2;
    const rsrc_t wor = mkrsrc(wo, H * DH * D * 2);
    const int cs = rq * 2 + (rg >> 2), i0 = (rg & 3) * 6;
#pragma unroll
    for (int t = 0; t < 6; ++t) { const int i = i0 + t; const unsigned d = (i >> 3) * 64 + cs * 8 + (i & 7); wot[t] = ld_nt(wor, (h * DH + d) * (D * 2) + colB); }
    const rsrc_t w2r = mkrsrc(w2, F * D * 2);
    w2t[0] = ld_nt(w2r, (grp * 12 + rg) * (D * 2) + colB);
    w2t[1] = ld_nt(w2r, rg < 4 ? (grp * 12 + 8 + rg) * (D * 2) + colB : OOB);
  };
  {
    GemvLoads g;
    float pa[4];
    if (j < 3) {
      const int p = lane >> 4, c4 = lane & 15;
      const unsigned o0 = WS_QO + ((h * 8 + p) * 192 + j * 64 + c4 * 4) * 4, o1 = o0 + 4 * 192 * 4;
      const unsigned om = wr16 ? WS_QO + (b * 192 + j * 64 + col4) * 4 : OOB;
      const u32x4 sn0 = ld_sc1(wsr, o0), sn1 = ld_sc1(wsr, o1), snm = ld_sc1(wsr, om);
      gemv_issue(g, xr, mkrsrc(wq, D * H * DH * 2), H * DH * 2, s * 80, (h * DH + j * 64) * 2, lane);
      __builtin_amdgcn_sched_barrier(0);
      if (tid < 12) (&L.qcnt)[tid] = 0u;
      if (tid == 12) L.tprod2 = 0u;
      WAIT_LKM0();
      __builtin_amdgcn_s_barrier();
      __builtin_amdgcn_sched_barrier(0);
      if (TLX < 0) load_tiles();
      __builtin_amdgcn_sched_barrier(0);
      const unsigned t0 = lsb4(sn0), t1 = lsb4(sn1);
      asm volatile("" :: "v"(t0), "v"(t1), "v"(snm.x) : "memory");
      if (lane == 0) lds_add(&L.pre);
      gemv_compute(g, pa, lane);
      STAMP(1);
      asm volatile("" :: "v"(pa[0]), "v"(pa[1]), "v"(pa[2]), "v"(pa[3]) : "memory");
      lds_wait_ge<0>(&L.pre, 4u);
      st_sc1(wsr, om, u32x4{(__float_as_uint(pa[0]) & ~1u) | (~snm.x & 1u), (__float_as_uint(pa[1]) & ~1u) | (~snm.y & 1u),
                            (__float_as_uint(pa[2]) & ~1u) | (~snm.z & 1u), (__float_as_uint(pa[3]) & ~1u) | (~snm.w & 1u)});
      u32x4 a0 = {0u, 0u, 0u, 0u}, a1 = {0u, 0u, 0u, 0u};
      bool k0 = false, k1 = false;
      __builtin_amdgcn_s_sleep(8);
#pragma nounroll
      for (unsigned spin = 0; spin < GSPIN; ++spin) {
        const unsigned mask = lds_ld(&L.qrdy);
        WAIT_LKM0();
        const u32x4 n0 = ld_sc1(wsr, k0 ? OOB : o0), n1 = ld_sc1(wsr, k1 ? OOB : o1);
        WAIT_VM0();
        if (!k0) { a0 = n0; k0 = (lsb4(n0) ^ t0) == 15u || ((mask >> p) & 1u); }
        if (!k1) { a1 = n1; k1 = (lsb4(n1) ^ t1) == 15u || ((mask >> (p + 4)) & 1u); }
        if (__all(k0 && k1)) break;
        __builtin_amdgcn_s_sleep(2);
      }
      float q[4];
#pragma unroll
      for (int k = 0; k < 4; ++k) { float t = __uint_as_float(a0[k] & ~1u) + __uint_as_float(a1[k] & ~1u); t = tr16(t, t); q[k] = tr32(t, t); }
      if (p == 0) {
        bf16x2 lo = {(__bf16)q[0], (__bf16)q[1]}, hi = {(__bf16)q[2], (__bf16)q[3]};
        *(u32x2*)&L.qs[j * 32 + c4 * 2] = u32x2{__builtin_bit_cast(unsigned, lo), __builtin_bit_cast(unsigned, hi)};
      }
      CFENCE();
      if (lane == 0) lds_add(&L.qcnt);
      STAMP(2);
      if (TLX >= 0 && j == 0) load_tiles();
    } else {
      const int unit = j == 3 ? b : ((b & 1) ? -1 : 256 + (b >> 1));
      const unsigned u = unit < 0 ? 0 : unit, mat = u / 192, lb = (u % 192) >> 3, rp = u & 7;
      const unsigned tw = lane < 8 ? WS_TG + (h * 8 + lane) * TGS : (lane >= 32 && s == 0) ? WS_TG + (lane - 32) * 8 * TGS : OOB;
      unsigned snap = 0;
      if (j == 4) {
        const u32x4 snap2 = ld_sc1(wsr, tw);
        snap = snap2.x;
        const u32x4 sv = {SENT, SENT, SENT, SENT};
        st_sc1(wsr, WS_P1 + b * 1024 + lane * 16, sv);
        st_sc1(wsr, b < 160 ? WS_P2 + b * 2048 + lane * 16 : OOB, sv);
        st_sc1(wsr, b < 160 ? WS_P2 + b * 2048 + 1024 + lane * 16 : OOB, sv);
        const unsigned u2 = lane < 16 ? b : 256 + (b >> 1);
        const bool on = lane < 16 || (lane < 32 && !(b & 1));
        st_sc1(wsr, on ? WS_FO + (((u2 / 192) * 8 + (u2 & 7)) * 1536 + ((u2 % 192) >> 3) * 64 + (lane & 15) * 4) * 4 : OOB, sv);
        __builtin_amdgcn_sched_barrier(0);
        __builtin_amdgcn_s_barrier();
        WAIT_VM0();
        if (lane < 8) { L.tsn[0][lane] = snap2.y; L.tsn[1][lane] = snap2.z; }
        CFENCE();
        if (lane == 0) lds_add(&L.pre);
      } else {
        __builtin_amdgcn_s_barrier();
      }
      gemv_issue(g, xr, mkrsrc(mat ? w3 : w1, unit < 0 ? 0 : D * F * 2), F * 2, rp * 80, lb * 128, lane);
      if (TLX < 0) load_tiles();
      if (j == 4) {
        const unsigned long long tl = rt();
#pragma nounroll
        for (unsigned spin = 0; spin < LSPIN; ++spin) {
          if (rfl(lds_ld(&L.qcnt)) >= 3u) break;
          __builtin_amdgcn_s_sleep(2);
          if (rt() - tl > 800ull && (spin & 15u) == 0u) {
            const unsigned cur = ld4_sc1(wsr, lane < 8 ? tw : OOB);
            const unsigned qm = (unsigned)__ballot(lane < 8 && ((cur ^ snap) & 3u));
            if (lane == 0) lds_st(&L.qrdy, qm);
          }
        }
        CFENCE();
        st4_sc1(wsr, lane == s ? tw : OOB, snap ^ 1u);
        STAMP(1);
        const unsigned want = s == 0 ? 3u : 4u;
        bool seen = s == 0 ? lane < 32 : lane != 0;
#pragma nounroll
        for (unsigned spin = 0; spin < GSPIN; ++spin) {
          __builtin_amdgcn_s_sleep(16);
          const unsigned cur = ld4_sc1(wsr, seen ? OOB : tw);
          seen = seen || ((cur ^ snap) & want) != 0u;
          if (__all(seen)) break;
        }
        st4_sc1(wsr, (s == 0 && lane == 0) ? tw : OOB, snap ^ 7u);
        if (lane == 0) lds_st(&L.bdone, 1u);
        STAMP(2);
      }
      gemv_compute(g, pa, lane);
      st_sc1(wsr, (wr16 && unit >= 0) ? WS_FO + ((mat * 8 + rp) * 1536 + lb * 64 + col4) * 4 : OOB, u32x4{san(pa[0]), san(pa[1]), san(pa[2]), san(pa[3])});
    }
  }
  float gate;
  {
    const u32x4 gxa = __builtin_amdgcn_raw_buffer_load_b128(xr, lane * 20, 0, 0);
    const unsigned gxb = __builtin_amdgcn_raw_buffer_load_b32(xr, lane * 20 + 16, 0, 0);
    const rsrc_t gr = mkrsrc(gw, D * 2);
    const u32x4 gwa = __builtin_amdgcn_raw_buffer_load_b128(gr, lane * 20, 0, 0);
    const unsigned gwb = __builtin_amdgcn_raw_buffer_load_b32(gr, lane * 20 + 16, 0, 0);
    float z = 0.f;
#pragma unroll
    for (int k = 0; k < 4; ++k) { z = fmaf(bf_lo(gxa[k]), bf_lo(gwa[k]), z); z = fmaf(bf_hi(gxa[k]), bf_hi(gwa[k]), z); }
    z = fmaf(bf_lo(gxb), bf_lo(gwb), z); z = fmaf(bf_hi(gxb), bf_hi(gwb), z);
    z = sum_hi3(sum_lo3(z));
    gate = 1.f / (1.f + __expf(-z));
  }
  if (j == 2) {
    unsigned at = XCHK;
#pragma nounroll
    for (int st = 0; st < (NDYN2 > 0 ? 2 : 1); ++st) {
      const unsigned pool = st ? NDYN2 : NDYN;
      lds_wait_ge<4>(&L.tcons, at);
      const unsigned old = L.tsn[st][lane & 7];
      const unsigned ta = WS_TG + (h * 8 + (lane & 7)) * TGS + 4 + st * 4;
      st4_sc1(wsr, lane == s ? ta : OOB, ((unsigned)rt() << 1) | (~old & 1u));
      unsigned tv = 0;
      bool seen = lane >= 8;
#pragma nounroll
      for (unsigned spin = 0; spin < GSPIN; ++spin) {
        __builtin_amdgcn_s_sleep(8);
        const unsigned cur = ld4_sc1(wsr, seen ? OOB : ta);
        if (!seen) { tv = cur; seen = ((cur ^ old) & 1u) != 0u; }
        if (__all(seen)) break;
      }
      const int rel = (int)((tv & ~1u) - (rfl(tv) & ~1u)) >> 1;
      const float lag = (float)rel - sum_lo3((float)rel) * 0.125f;
      float n = fmaxf((float)pool * 0.125f - lag * RATE, 0.f);
      n *= (float)pool / sum_lo3(n);
      unsigned cum = (unsigned)(n * 16.f);
      { unsigned t = udpp<SHR1>(cum); cum += (lane & 7) >= 1 ? t : 0u; t = udpp<SHR2>(cum); cum += (lane & 7) >= 2 ? t : 0u; t = udpp<SHR4>(cum); cum += (lane & 7) >= 4 ? t : 0u; }
      const unsigned hi = (lane & 7) == 7 ? pool : min((cum + 8u) >> 4, pool);
      const unsigned hprev = udpp<SHR1>(hi);
      const unsigned lo = (lane & 7) == 0 ? 0u : hprev;
      if (lane == s) lds_st(st ? &L.tprod2 : &L.tprod, 0x80000000u | (lo << 12) | (hi - lo));
      at = NSW + __builtin_amdgcn_readlane(hi - lo, s) - R2;
    }
  }
  if (j == 3) {
    const int part = lane >> 4, mr = lane & 15;
    const unsigned o = part < 3 ? WS_FO + (mr * 1536 + grp * 12 + part * 4) * 4 : OOB;
    lds_wait_ge<8>(&L.bdone, 1u);
#pragma nounroll
    while (rt() - tstart < FFN_T && !rfl(lds_ld(&L.sflag[0]))) __builtin_amdgcn_s_sleep(32);
    u32x4 a;
#pragma nounroll
    for (unsigned spin = 0; spin < GSPIN; ++spin) {
      CFENCE();
      a = ld_sc1(wsr, o);
      if (__all(valid4(a))) break;
      __builtin_amdgcn_s_sleep(16);
    }
    float hv[4];
#pragma unroll
    for (int k = 0; k < 4; ++k) {
      const float t = sum_lo3(__uint_as_float(a[k]));
      const float o8 = dpp<XOR8>(t);
      const float gv = (mr & 8) ? o8 : t, uv = (mr & 8) ? t : o8;
      hv[k] = gv / (1.f + __expf(-gv)) * uv;
    }
    if (mr == 0) *(f32x4*)&L.hs[part * 4] = f32x4{hv[0], hv[1], hv[2], hv[3]};
    CFENCE();
    if (lane == 0) lds_st(&L.hrdy, 1u);
  }
  if (TLX >= 0 && j != 0) {
    if (TLX < 1000) {
      lds_wait_ge<8>(&L.tprod2, 0x80000000u);
      const unsigned tot = NSW + (rfl(lds_ld(&L.tprod)) & 0xfffu) + (rfl(lds_ld(&L.tprod2)) & 0xfffu);
      lds_wait_ge<1>(&L.tcons, tot + 4 - TLX);
    } else {
#pragma nounroll
      for (unsigned spin = 0; spin < LSPIN; ++spin) {
        const u32x4 f = *(volatile u32x4*)&L.sflag[0];
        if (rfl(f.x | f.y | f.z | f.w)) break;
        __builtin_amdgcn_s_sleep(2);
      }
      CFENCE();
    }
    load_tiles();
  }
  f32x2 wf[6][4];
#pragma unroll
  for (int t = 0; t < 6; ++t)
#pragma unroll
    for (int k = 0; k < 4; ++k) { wf[t][k] = f32x2{bf_lo(wot[t][k]), bf_hi(wot[t][k])}; asm volatile("" : "+v"(wf[t][k])); }
  lds_wait_ge<8>(&L.hrdy, 1u);
  f32x2 y2[4];
  {
    const float h0 = L.hs[rg] * (1.f - gate), h1 = L.hs[8 + rg] * (1.f - gate);
#pragma unroll
    for (int k = 0; k < 4; ++k) y2[k] = f32x2{h0 * bf_lo(w2t[0][k]) + h1 * bf_lo(w2t[1][k]), h0 * bf_hi(w2t[0][k]) + h1 * bf_hi(w2t[1][k])};
  }
  STAMP(3);
  if (j == 0) {
#pragma nounroll
    for (unsigned spin = 0; spin < LSPIN; ++spin) {
      const u32x4 f = *(volatile u32x4*)&L.sflag[0];
      if (rfl(f.x & f.y & f.z & f.w)) break;
      if (!rfl(f.x | f.y | f.z | f.w)) __builtin_amdgcn_s_sleep(2);
    }
    CFENCE();
    STAMP(4);
    {
      const int hf = lane & 1;
      f32x4 pv[NSTR]; f32x2 pm[NSTR];
#pragma unroll
      for (int q2 = 0; q2 < NSTR; ++q2) { pv[q2] = *(const f32x4*)&L.mrg[q2][lane * 4]; pm[q2] = *(const f32x2*)&L.ml[q2][0]; }
      float MM = pm[0].x;
#pragma unroll
      for (int q2 = 1; q2 < NSTR; ++q2) MM = fmaxf(MM, pm[q2].x);
      float o0 = 0.f, o1 = 0.f, o2 = 0.f, o3 = 0.f;
#pragma unroll
      for (int q2 = 0; q2 < NSTR; ++q2) {
        const float ww = __builtin_amdgcn_exp2f(pm[q2].x - MM);
        o0 = fmaf(ww, pv[q2].x, o0); o1 = fmaf(ww, pv[q2].y, o1);
        o2 = fmaf(ww, pv[q2].z, o2); o3 = fmaf(ww, hf ? pm[q2].y : pv[q2].w, o3);
      }
      if (hf) o2 = MM;
      st_sc1(wsr, WS_P1 + b * 1024 + lane * 16, u32x4{san(o0), san(o1), san(o2), san(o3)});
    }
    STAMP(7);
    const unsigned o = WS_P1 + (h * 8 + c) * 1024 + (rq * 8 + rg) * 32;
    u32x4 A = {0u, 0u, 0u, 0u}, B = {0u, 0u, 0u, 0u};
    bool ok = false;
#pragma nounroll
    for (unsigned spin = 0; spin < GSPIN; ++spin) {
      CFENCE();
      const u32x4 nA = ld_sc1(wsr, ok ? OOB : o), nB = ld_sc1(wsr, ok ? OOB : o + 16);
      if (!ok) { A = nA; B = nB; ok = valid4(nA) && valid4(nB); }
      if (__all(ok)) break;
    }
    STAMP(5);
    const float mp = __uint_as_float(B.z);
    const float M = max_lo3(mp);
    const float w = __builtin_amdgcn_exp2f(mp - M);
    const float sc = gate * __builtin_amdgcn_rcpf(sum_lo3(w * __uint_as_float(B.w)));
    const float o0 = sum_lo3(w * __uint_as_float(A.x)) * sc, o1 = sum_lo3(w * __uint_as_float(A.y)) * sc, o2 = sum_lo3(w * __uint_as_float(A.z)) * sc;
    const float o3 = sum_lo3(w * __uint_as_float(A.w)) * sc, o4 = sum_lo3(w * __uint_as_float(B.x)) * sc, o5 = sum_lo3(w * __uint_as_float(B.y)) * sc;
    if (c == 0) { *(f32x4*)&L.os[rg * 8] = f32x4{o0, o1, o2, o3}; *(f32x2*)&L.os[rg * 8 + 4] = f32x2{o4, o5}; }
    CFENCE();
    if (lane == 0) lds_st(&L.oflag, 1u);
    CFENCE();
  } else {
    lds_wait_ge<4>(&L.sflag[0], 1u);
    lds_wait_ge<0>(&L.oflag, 1u);
  }
  {
    const f32x4 oa = *(const f32x4*)&L.os[rg * 8];
    const f32x2 ob = *(const f32x2*)&L.os[rg * 8 + 4];
#pragma unroll
    for (int t = 0; t < 6; ++t) {
      const float ov = t < 4 ? oa[t] : ob[t - 4];
#pragma unroll
      for (int k = 0; k < 4; ++k) y2[k] = __builtin_elementwise_fma(f32x2{ov, ov}, wf[t][k], y2[k]);
    }
    float r4[4];
#pragma unroll
    for (int k = 0; k < 4; ++k) { float t = tr32(y2[k >> 1][k & 1], y2[2 + (k >> 1)][k & 1]); t = tr16(t, t); r4[k] = t + dpp<XOR8>(t); }
    st_sc1(wsr, wr16 ? WS_P2 + (half * 80 + j * 16 + (col4 >> 2)) * 2048 + grp * 16 : OOB, u32x4{san(r4[0]), san(r4[1]), san(r4[2]), san(r4[3])});
  }
  STAMP(6);
  if (j == 1 && b < 160) {
    const unsigned o0 = WS_P2 + b * 2048 + lane * 16;
    u32x4 a0 = {0u, 0u, 0u, 0u}, a1 = {0u, 0u, 0u, 0u};
    bool k0 = false, k1 = false;
#pragma nounroll
    for (unsigned spin = 0; spin < GSPIN; ++spin) {
      CFENCE();
      const u32x4 n0 = ld_sc1(wsr, k0 ? OOB : o0), n1 = ld_sc1(wsr, k1 ? OOB : o0 + 1024);
      if (!k0) { a0 = n0; k0 = valid4(n0); }
      if (!k1) { a1 = n1; k1 = valid4(n1); }
      if (__all(k0 && k1)) break;
    }
    const float v0 = __uint_as_float(a0.x) + __uint_as_float(a1.x), v1 = __uint_as_float(a0.y) + __uint_as_float(a1.y);
    const float v2 = __uint_as_float(a0.z) + __uint_as_float(a1.z), v3 = __uint_as_float(a0.w) + __uint_as_float(a1.w);
    float t = tr16(tr32(v0, v2), tr32(v1, v3));
    t += dpp<XOR8>(t); t = sum_lo3(t);
    const __bf16 ov = (__bf16)t;
    __builtin_amdgcn_raw_buffer_store_b16(__builtin_bit_cast(unsigned short, ov), mkrsrc(out, D * 2), (lane & 15) == 0 ? (b * 4 + (lane >> 4)) * 2 : OOB, 0, 0);
    STAMP(7);
  }
  STAMP_FLUSH();
}

extern "C" void kernel_launch(void* const* d_in, const int* in_sizes, int n_in, void* d_out, int out_size, void* d_ws, size_t ws_size,
                              hipStream_t stream) {
  fused_kernel<<<256, NTHR, 0, stream>>>((const uint16_t*)d_in[0], (const uint16_t*)d_in[1], (const uint16_t*)d_in[2], (const uint16_t*)d_in[3],
                                         (const uint16_t*)d_in[4], (const uint16_t*)d_in[6], (const uint16_t*)d_in[7], (const uint16_t*)d_in[8],
                                         (const uint16_t*)d_in[9], (unsigned char*)d_ws, (uint16_t*)d_out);
}
```

```cpp
#include <hip/hip_runtime.h>
#include <hip/hip_bf16.h>
#include <stdint.h>
#include <stddef.h>

typedef unsigned u32x4 __attribute__((ext_vector_type(4)));
typedef unsigned u32x2 __attribute__((ext_vector_type(2)));
typedef float f32x4 __attribute__((ext_vector_type(4)));
typedef float f32x2 __attribute__((ext_vector_type(2)));
typedef __bf16 bf16x2 __attribute__((ext_vector_type(2)));
typedef __amdgpu_buffer_rsrc_t rsrc_t;

#define RING 4
#define AHEAD 1
#define INF 1
#define PMAX 6
#define NDYNU 128
#define XCHK 28
#define RATE 0.05f
#define TLX 1000
#define NDYN2U 64
#define R2 16
#define FFN_T 2000

constexpr int D = 640, H = 32, SK = 8192, DH = 192, F = 1536;
constexpr int NHELP = 5, NSTR = 4, NWAVES = NHELP + NSTR, NTHR = NWAVES * 64;
constexpr int AUX_NT = 2, AUX_SC1 = 16;
constexpr unsigned OOB = 0x7ffffff0u;
constexpr unsigned WS_QO = 0;
constexpr unsigned WS_FO = WS_QO + 256 * 192 * 4;
constexpr unsigned WS_P1 = WS_FO + 16 * 1536 * 4;
constexpr unsigned WS_P2 = WS_P1 + 256 * 1024;
constexpr unsigned WS_TG = WS_P2 + 160 * 2048;
constexpr unsigned TGS = 128;
constexpr unsigned WS_END = WS_TG + 256 * TGS;
constexpr unsigned WS_STAMP = 2u << 20;
constexpr unsigned SENT = 0xFFFFFFFFu;
constexpr unsigned NWH = 8 * NSTR, NDYN = NDYNU, NDYN2 = NDYN2U, STAT = 1024 - NDYN - NDYN2, UPW = STAT / 8;
constexpr unsigned R0 = RING + PMAX + AHEAD;
constexpr unsigned NSW = UPW - R0 * NSTR;
static_assert(STAT % NWH == 0 && UPW >= R0 * NSTR && AHEAD < RING && INF <= RING && NDYN > 0 && XCHK <= NSW && R2 <= NSW - XCHK, "work split");

__device__ __forceinline__ float bf_lo(unsigned u) { return __builtin_bit_cast(float, u << 16); }
__device__ __forceinline__ float bf_hi(unsigned u) { return __builtin_bit_cast(float, u & 0xffff0000u); }
__device__ __forceinline__ float dot2(unsigned k, unsigned q, float acc) {
  return __builtin_amdgcn_fdot2_f32_bf16(__builtin_bit_cast(bf16x2, k), __builtin_bit_cast(bf16x2, q), acc, false);
}
template <int CTRL> __device__ __forceinline__ float dpp(float x) {
  return __builtin_bit_cast(float, __builtin_amdgcn_mov_dpp(__builtin_bit_cast(int, x), CTRL, 0xf, 0xf, true));
}
template <int CTRL> __device__ __forceinline__ unsigned udpp(unsigned x) { return (unsigned)__builtin_amdgcn_mov_dpp((int)x, CTRL, 0xf, 0xf, true); }
constexpr int XOR1 = 0xB1, XOR2 = 0x4E, HMIR = 0x141, XOR8 = 0x128, SHR1 = 0x111, SHR2 = 0x112, SHR4 = 0x114;
__device__ __forceinline__ float tr16(float a, float b) {
  auto s = __builtin_amdgcn_permlane16_swap(__float_as_uint(a), __float_as_uint(b), false, false);
  return __uint_as_float(s[0]) + __uint_as_float(s[1]);
}
__device__ __forceinline__ float tr32(float a, float b) {
  auto s = __builtin_amdgcn_permlane32_swap(__float_as_uint(a), __float_as_uint(b), false, false);
  return __uint_as_float(s[0]) + __uint_as_float(s[1]);
}
__device__ __forceinline__ float max16(float x) {
  auto s = __builtin_amdgcn_permlane16_swap(__float_as_uint(x), __float_as_uint(x), false, false);
  return fmaxf(__uint_as_float(s[0]), __uint_as_float(s[1]));
}
__device__ __forceinline__ float max32(float x) {
  auto s = __builtin_amdgcn_permlane32_swap(__float_as_uint(x), __float_as_uint(x), false, false);
  return fmaxf(__uint_as_float(s[0]), __uint_as_float(s[1]));
}
__device__ __forceinline__ float sum_hi3(float x) { x += dpp<XOR8>(x); x = tr16(x, x); return tr32(x, x); }
__device__ __forceinline__ float sum_lo3(float x) { x += dpp<XOR1>(x); x += dpp<XOR2>(x); x += dpp<HMIR>(x); return x; }
__device__ __forceinline__ float max_lo3(float x) { x = fmaxf(x, dpp<XOR1>(x)); x = fmaxf(x, dpp<XOR2>(x)); return fmaxf(x, dpp<HMIR>(x)); }
__device__ __forceinline__ unsigned san(float v) { unsigned u = __float_as_uint(v); return u < 0xFFFFFFFEu ? u : 0xFFFFFFFEu; }
__device__ __forceinline__ rsrc_t mkrsrc(const void* p, unsigned bytes) { return __builtin_amdgcn_make_buffer_rsrc((void*)p, 0, bytes, 0x00020000); }
__device__ __forceinline__ u32x4 ld_nt(rsrc_t r, unsigned voff) { return __builtin_amdgcn_raw_buffer_load_b128(r, voff, 0, AUX_NT); }
__device__ __forceinline__ u32x4 ld_sc1(rsrc_t r, unsigned voff) { return __builtin_amdgcn_raw_buffer_load_b128(r, voff, 0, AUX_SC1); }
__device__ __forceinline__ unsigned ld4_sc1(rsrc_t r, unsigned voff) { return __builtin_amdgcn_raw_buffer_load_b32(r, voff, 0, AUX_SC1); }
__device__ __forceinline__ void st_sc1(rsrc_t r, unsigned voff, u32x4 v) { __builtin_amdgcn_raw_buffer_store_b128(v, r, voff, 0, AUX_SC1); }
__device__ __forceinline__ void st4_sc1(rsrc_t r, unsigned voff, unsigned v) { __builtin_amdgcn_raw_buffer_store_b32(v, r, voff, 0, AUX_SC1); }
__device__ __forceinline__ bool valid4(u32x4 v) { return v.x != SENT && v.y != SENT && v.z != SENT && v.w != SENT; }
__device__ __forceinline__ unsigned lsb4(u32x4 v) { return (v.x & 1u) | ((v.y & 1u) << 1) | ((v.z & 1u) << 2) | ((v.w & 1u) << 3); }
__device__ __forceinline__ unsigned long long rt() { return __builtin_amdgcn_s_memrealtime(); }
__device__ __forceinline__ unsigned lds_ld(const unsigned* p) { return __hip_atomic_load(p, __ATOMIC_RELAXED, __HIP_MEMORY_SCOPE_WORKGROUP); }
__device__ __forceinline__ void lds_st(unsigned* p, unsigned v) { __hip_atomic_store(p, v, __ATOMIC_RELAXED, __HIP_MEMORY_SCOPE_WORKGROUP); }
__device__ __forceinline__ unsigned lds_add(unsigned* p) { return __hip_atomic_fetch_add(p, 1u, __ATOMIC_RELAXED, __HIP_MEMORY_SCOPE_WORKGROUP); }
__device__ __forceinline__ unsigned rfl(unsigned v) { return __builtin_amdgcn_readfirstlane(v); }
constexpr unsigned GSPIN = 1u << 15;
constexpr unsigned LSPIN = 1u << 19;
template <int SLEEP> __device__ __forceinline__ void lds_wait_ge(const unsigned* p, unsigned v) {
#pragma nounroll
  for (unsigned spin = 0; spin < LSPIN; ++spin) {
    if (rfl(lds_ld(p)) >= v) break;
    __builtin_amdgcn_s_sleep(SLEEP);
  }
  asm volatile("" ::: "memory");
}
#define WAIT_VM0() asm volatile("s_waitcnt vmcnt(0)" ::: "memory")
#define WAIT_LKM0() asm volatile("s_waitcnt lgkmcnt(0)" ::: "memory")
#define CFENCE() asm volatile("" ::: "memory")

#define STAMP(i) do {} while (0)
#define STAMP_FLUSH() do {} while (0)

template <int N> struct IC { static constexpr int value = N; };
template <int B, int E, class Fn> __device__ __forceinline__ void static_for(Fn&& f) { if constexpr (B < E) { f(IC<B>{}); static_for<B + 1, E>(f); } }

struct Lds {
  float mrg[NSTR][256];
  float ml[NSTR][2];
  unsigned qs[96];
  float os[64];
  float hs[16];
  unsigned qcnt;
  unsigned pre;
  unsigned qrdy;
  unsigned bdone;
  unsigned hrdy;
  unsigned oflag;
  unsigned tcons;
  unsigned tprod;
  unsigned sflag[4];
  unsigned tsn[2][8];
  unsigned tprod2, pad2[3];
  u32x4 park[NSTR][PMAX][6][64];
};
static_assert(offsetof(Lds, sflag) % 16 == 0 && offsetof(Lds, os) % 16 == 0 && offsetof(Lds, qs) % 16 == 0 && offsetof(Lds, hs) % 16 == 0 && offsetof(Lds, park) % 16 == 0, "LDS alignment");
static_assert(sizeof(Lds) <= 160 * 1024, "LDS size");

struct GemvLoads { u32x4 w[10]; unsigned xs; };
__device__ __forceinline__ void gemv_issue(GemvLoads& g, rsrc_t xr, rsrc_t wr, unsigned ldB, unsigned row0, unsigned colB, int lane) {
  const unsigned off = (row0 + (lane >> 3)) * ldB + colB + (lane & 7) * 16;
#pragma unroll
  for (int i = 0; i < 10; ++i) g.w[i] = ld_nt(wr, off + i * 8 * ldB);
  g.xs = __builtin_amdgcn_raw_buffer_load_b32(xr, lane < 40 ? row0 * 2 + lane * 4 : OOB, 0, 0);
}
__device__ __forceinline__ void gemv_compute(const GemvLoads& g, float (&r)[4], int lane) {
  float acc[8];
#pragma unroll
  for (int k = 0; k < 8; ++k) acc[k] = 0.f;
  const int rg = lane >> 3;
#pragma unroll
  for (int i = 0; i < 10; ++i) {
    const unsigned xw = (unsigned)__builtin_amdgcn_ds_bpermute((i * 4 + (rg >> 1)) * 4, (int)g.xs);
    const float xv = (rg & 1) ? bf_hi(xw) : bf_lo(xw);
#pragma unroll
    for (int k = 0; k < 4; ++k) { acc[2 * k] = fmaf(xv, bf_lo(g.w[i][k]), acc[2 * k]); acc[2 * k + 1] = fmaf(xv, bf_hi(g.w[i][k]), acc[2 * k + 1]); }
  }
#pragma unroll
  for (int k = 0; k < 4; ++k) { float t = tr32(acc[k], acc[k + 4]); t = tr16(t, t); r[k] = t + dpp<XOR8>(t); }
}

__global__ void __launch_bounds__(NTHR) fused_kernel(const uint16_t* __restrict__ x, const uint16_t* __restrict__ kc, const uint16_t* __restrict__ vc,
                                                     const uint16_t* __restrict__ wq, const uint16_t* __restrict__ wo, const uint16_t* __restrict__ w1,
                                                     const uint16_t* __restrict__ w3, const uint16_t* __restrict__ w2, const uint16_t* __restrict__ gw,
                                                     unsigned char* __restrict__ ws, uint16_t* __restrict__ out) {
  __shared__ __attribute__((aligned(16))) Lds L;
  const int b = blockIdx.x, h = b >> 3, s = b & 7;
  const int tid = threadIdx.x, lane = tid & 63, wave = __builtin_amdgcn_readfirstlane(tid >> 6);
  const int rg = lane >> 3, c = lane & 7;
  STAMP(0);
  asm volatile("" :: "s"(x), "s"(kc), "s"(vc), "s"(wq), "s"(wo), "s"(w1), "s"(w3), "s"(w2), "s"(gw), "s"(ws), "s"(out));
  const rsrc_t wsr = mkrsrc(ws, WS_END);

  if (wave >= NHELP) {
    const int sw = wave - NHELP;
    const unsigned long long kb = (unsigned long long)(kc + (size_t)h * SK * DH), vb = (unsigned long long)(vc + (size_t)h * SK * DH);
    const u32x4 kr = {(unsigned)kb, (unsigned)(kb >> 32) & 0xffffu, (unsigned)(SK * DH * 2), 0x00020000u};
    const u32x4 vr = {(unsigned)vb, (unsigned)(vb >> 32) & 0xffffu, (unsigned)(SK * DH * 2), 0x00020000u};
    const unsigned loff = (unsigned)(rg * DH + c * 8) * 2;
    constexpr unsigned STEPB = 8 * DH * 2;
    u32x4 K[RING][3], V[RING][3];
#define RING_LOADS "s_nop 4\n\t" \
                   "buffer_load_dwordx4 %0, %6, %7, %9 offen nt\n\t" \
                   "buffer_load_dwordx4 %1, %6, %7, %9 offen offset:128 nt\n\t" \
                   "buffer_load_dwordx4 %2, %6, %7, %9 offen offset:256 nt\n\t" \
                   "buffer_load_dwordx4 %3, %6, %8, %9 offen\n\t" \
                   "buffer_load_dwordx4 %4, %6, %8, %9 offen offset:128\n\t" \
                   "buffer_load_dwordx4 %5, %6, %8, %9 offen offset:256"
    auto issue0 = [&](int slot, unsigned unit) {
      const unsigned so = unit * STEPB;
      asm volatile(RING_LOADS
                   : "=&v"(K[slot][0]), "=&v"(K[slot][1]), "=&v"(K[slot][2]), "=&v"(V[slot][0]), "=&v"(V[slot][1]), "=&v"(V[slot][2])
                   : "v"(loff), "s"(kr), "s"(vr), "s"(so));
    };
    auto issue = [&](int slot, unsigned unit) {
      const unsigned so = unit * STEPB;
      asm volatile(RING_LOADS
                   : "+v"(K[slot][0]), "+v"(K[slot][1]), "+v"(K[slot][2]), "+v"(V[slot][0]), "+v"(V[slot][1]), "+v"(V[slot][2])
                   : "v"(loff), "s"(kr), "s"(vr), "s"(so));
    };
#pragma unroll
    for (int i = 0; i < RING; ++i) {
      if (i >= INF) asm volatile("s_waitcnt vmcnt(%0)" :: "i"((INF - 1) * 6) : "memory");
      issue0(i, i * NWH + s * NSTR + sw);
      __builtin_amdgcn_sched_barrier(0);
      if (i == INF - 1) { __builtin_amdgcn_s_barrier(); __builtin_amdgcn_sched_barrier(0); }
    }
    unsigned pi = RING;
    auto private_unit = [&]() -> unsigned { return (pi++) * NWH + s * NSTR + sw; };
    int np = 0;
    {
      const unsigned pbase = rfl((unsigned)(size_t)&L.park[sw][0][0][0]);
#pragma nounroll
      while (np < PMAX) {
        asm volatile("s_waitcnt vmcnt(%0)" :: "i"((INF - 1) * 6) : "memory");
        if (rfl(lds_ld(&L.qcnt)) >= 3u) break;
        const unsigned so = private_unit() * STEPB, mb = pbase + np * 6144;
        unsigned keep;
        asm volatile("s_mov_b32 %0, m0\n\t"
                     "s_mov_b32 m0, %1\n\ts_nop 4\n\t"
                     "buffer_load_dwordx4 %2, %3, %5 offen nt lds\n\t"
                     "s_add_u32 m0, %1, 896\n\ts_nop 0\n\t"
                     "buffer_load_dwordx4 %2, %3, %5 offen offset:128 nt lds\n\t"
                     "s_add_u32 m0, %1, 1792\n\ts_nop 0\n\t"
                     "buffer_load_dwordx4 %2, %3, %5 offen offset:256 nt lds\n\t"
                     "s_add_u32 m0, %1, 3072\n\ts_nop 0\n\t"
                     "buffer_load_dwordx4 %2, %4, %5 offen lds\n\t"
                     "s_add_u32 m0, %1, 3968\n\ts_nop 0\n\t"
                     "buffer_load_dwordx4 %2, %4, %5 offen offset:128 lds\n\t"
                     "s_add_u32 m0, %1, 4864\n\ts_nop 0\n\t"
                     "buffer_load_dwordx4 %2, %4, %5 offen offset:256 lds\n\t"
                     "s_mov_b32 m0, %0"
                     : "=&s"(keep) : "s"(mb), "v"(loff), "s"(kr), "s"(vr), "s"(so) : "scc", "memory");
        ++np;
      }
    }
    STAMP(1);
    constexpr float C2 = 0.07216878364870322f * 1.4426950408889634f;
    float m = -INFINITY, l = 0.f, p_, alpha_;
    f32x2 acc2[12];
#pragma unroll
    for (int i = 0; i < 12; ++i) acc2[i] = f32x2{0.f, 0.f};
    u32x4 qp[3];
    auto scores = [&](const u32x4 (&Kx)[3]) {
      float d3[3];
#pragma unroll
      for (int jj = 0; jj < 3; ++jj) {
        d3[jj] = 0.f;
#pragma unroll
        for (int k = 0; k < 4; ++k) d3[jj] = dot2(Kx[jj][k], qp[jj][k], d3[jj]);
      }
      const float sc = sum_lo3(d3[0] + d3[1] + d3[2]) * C2;
      float pm = fmaxf(sc, dpp<XOR8>(sc)); pm = max32(max16(pm));
      const float mn = fmaxf(m, pm);
      alpha_ = __builtin_amdgcn_exp2f(m - mn); p_ = __builtin_amdgcn_exp2f(sc - mn);
      l = fmaf(l, alpha_, p_); m = mn;
    };
    auto accum = [&](const u32x4 (&Vx)[3]) {
      if (rfl(__float_as_uint(alpha_)) != 0x3f800000u) {
#pragma unroll
        for (int i = 0; i < 12; ++i) acc2[i] *= f32x2{alpha_, alpha_};
      }
#pragma unroll
      for (int jj = 0; jj < 3; ++jj)
#pragma unroll
        for (int k = 0; k < 4; ++k) acc2[jj * 4 + k] = __builtin_elementwise_fma(f32x2{p_, p_}, f32x2{bf_lo(Vx[jj][k]), bf_hi(Vx[jj][k])}, acc2[jj * 4 + k]);
#pragma unroll
      for (int jj = 0; jj < 3; ++jj) asm volatile("" : "+v"(acc2[jj * 4]), "+v"(acc2[jj * 4 + 1]), "+v"(acc2[jj * 4 + 2]), "+v"(acc2[jj * 4 + 3]));
    };
    auto consume = [&](int slot, auto younger) {
      constexpr int YN = decltype(younger)::value * 6;
      asm volatile("s_waitcnt vmcnt(%3)" : "+v"(K[slot][0]), "+v"(K[slot][1]), "+v"(K[slot][2]) : "i"(YN + 3));
      scores(K[slot]);
      asm volatile("s_waitcnt vmcnt(%3)" : "+v"(V[slot][0]), "+v"(V[slot][1]), "+v"(V[slot][2]) : "i"(YN));
      accum(V[slot]);
    };
    auto consume_parked = [&](int ip) {
      u32x4 T[3];
#pragma unroll
      for (int jj = 0; jj < 3; ++jj) T[jj] = L.park[sw][ip][jj][lane];
      scores(T);
      __builtin_amdgcn_sched_barrier(0);
#pragma unroll
      for (int jj = 0; jj < 3; ++jj) T[jj] = L.park[sw][ip][3 + jj][lane];
      accum(T);
    };
    lds_wait_ge<1>(&L.qcnt, 3u);
#pragma unroll
    for (int jj = 0; jj < 3; ++jj) qp[jj] = *(const u32x4*)&L.qs[jj * 32 + c * 4];
    STAMP(2);
    static_for<0, RING>([&](auto ic) {
      constexpr int i = decltype(ic)::value;
      consume(i, IC<RING - 1 - i + (i < AHEAD ? i : AHEAD)>{});
      if constexpr (i < AHEAD) issue(i, private_unit());
      __builtin_amdgcn_sched_barrier(0);
    });
    int rem = AHEAD;
    bool more = true;
#pragma nounroll
    do {
#pragma unroll
      for (int k = 0; k < RING; ++k) {
        if (rem > 0) {
          unsigned un = 1024u;
          if (pi < R0) un = private_unit();
          else if (more) {
            unsigned idx = 0;
            if (lane == 0) idx = lds_add(&L.tcons);
            idx = rfl(idx);
            if (idx < NSW) un = (R0 + idx / NSTR) * NWH + s * NSTR + idx % NSTR;
            else {
              idx -= NSW;
#pragma nounroll
              for (unsigned spin = 0; spin < LSPIN; ++spin) {
                const unsigned pr = rfl(lds_ld(&L.tprod));
                if (pr >> 31) {
                  if (idx < (pr & 0xfffu)) { un = STAT + ((pr >> 12) & 0xfffu) + idx; break; }
                  if (NDYN2 == 0) break;
                  const unsigned p2 = rfl(lds_ld(&L.tprod2)), i2 = idx - (pr & 0xfffu);
                  if (p2 >> 31) { if (i2 < (p2 & 0xfffu)) un = STAT + NDYN + ((p2 >> 12) & 0xfffu) + i2; break; }
                }
                __builtin_amdgcn_s_sleep(1);
              }
            }
          }
          more = un < 1024u;
          issue((k + AHEAD) % RING, un);
          rem += more ? 0 : -1;
          consume(k, IC<AHEAD>{});
          __builtin_amdgcn_sched_barrier(0);
        }
      }
      if (np > 0) { CFENCE(); --np; consume_parked(np); __builtin_amdgcn_sched_barrier(0); }
    } while (rem > 0 || np > 0);
#pragma unroll
    for (int r2 = 0; r2 < RING; ++r2)
      asm volatile("s_waitcnt vmcnt(0)" :: "v"(K[r2][0]), "v"(K[r2][1]), "v"(K[r2][2]), "v"(V[r2][0]), "v"(V[r2][1]), "v"(V[r2][2]));
    __builtin_amdgcn_sched_barrier(0);
    STAMP(3);
    float r6[6];
    {
      float r12[12];
#pragma unroll
      for (int i = 0; i < 12; ++i) r12[i] = tr32(acc2[i >> 1][i & 1], acc2[6 + (i >> 1)][i & 1]);
#pragma unroll
      for (int i = 0; i < 6; ++i) { const float t = tr16(r12[i], r12[i + 6]); r6[i] = t + dpp<XOR8>(t); }
    }
    const float lw = sum_hi3(l);
    if ((lane & 8) == 0) {
      const int slot = c * 4 + (lane >> 4);
      *(f32x4*)&L.mrg[sw][slot * 8] = f32x4{r6[0], r6[1], r6[2], r6[3]};
      *(f32x2*)&L.mrg[sw][slot * 8 + 4] = f32x2{r6[4], r6[5]};
    } else if (lane == 8) *(f32x2*)&L.ml[sw][0] = f32x2{m, lw};
    CFENCE();
    if (lane == 0) lds_st(&L.sflag[sw], 1u);
    STAMP(4);
    STAMP_FLUSH();
    return;
  }

  const unsigned long long tstart = rt();
  const rsrc_t xr = mkrsrc(x, D * 2);
  const int j = wave;
  const int rq = s >> 1, half = s & 1;
  const bool wr16 = (lane & 0x18) == 0;
  const unsigned col4 = c * 8 + (lane >> 5) * 4;
  const unsigned grp = h * 4 + rq;
  u32x4 wot[6], w2t[2];
#pragma unroll
  for (int t = 0; t < 6; ++t) wot[t] = u32x4{0u, 0u, 0u, 0u};
  w2t[0] = w2t[1] = u32x4{0u, 0u, 0u, 0u};
  auto load_tiles = [&]() {
    const unsigned colB = (half * 320 + j * 64 + c * 8) * 2;
    const rsrc_t wor = mkrsrc(wo, H * DH * D * 2);
    const int cs = rq * 2 + (rg >> 2), i0 = (rg & 3) * 6;
#pragma unroll
    for (int t = 0; t < 6; ++t) { const int i = i0 + t; const unsigned d = (i >> 3) * 64 + cs * 8 + (i & 7); wot[t] = ld_nt(wor, (h * DH + d) * (D * 2) + colB); }
    const rsrc_t w2r = mkrsrc(w2, F * D * 2);
    w2t[0] = ld_nt(w2r, (grp * 12 + rg) * (D * 2) + colB);
    w2t[1] = ld_nt(w2r, rg < 4 ? (grp * 12 + 8 + rg) * (D * 2) + colB : OOB);
  };
  {
    GemvLoads g;
    float pa[4];
    if (j < 3) {
      const int p = lane >> 4, c4 = lane & 15;
      const unsigned o0 = WS_QO + ((h * 8 + p) * 192 + j * 64 + c4 * 4) * 4, o1 = o0 + 4 * 192 * 4;
      const unsigned om = wr16 ? WS_QO + (b * 192 + j * 64 + col4) * 4 : OOB;
      const u32x4 sn0 = ld_sc1(wsr, o0), sn1 = ld_sc1(wsr, o1), snm = ld_sc1(wsr, om);
      gemv_issue(g, xr, mkrsrc(wq, D * H * DH * 2), H * DH * 2, s * 80, (h * DH + j * 64) * 2, lane);
      __builtin_amdgcn_sched_barrier(0);
      if (tid < 12) (&L.qcnt)[tid] = 0u;
      if (tid == 12) L.tprod2 = 0u;
      WAIT_LKM0();
      __builtin_amdgcn_s_barrier();
      __builtin_amdgcn_sched_barrier(0);
      if (TLX < 0) load_tiles();
      __builtin_amdgcn_sched_barrier(0);
      const unsigned t0 = lsb4(sn0), t1 = lsb4(sn1);
      asm volatile("" :: "v"(t0), "v"(t1), "v"(snm.x) : "memory");
      if (lane == 0) lds_add(&L.pre);
      gemv_compute(g, pa, lane);
      STAMP(1);
      asm volatile("" :: "v"(pa[0]), "v"(pa[1]), "v"(pa[2]), "v"(pa[3]) : "memory");
      lds_wait_ge<0>(&L.pre, 4u);
      st_sc1(wsr, om, u32x4{(__float_as_uint(pa[0]) & ~1u) | (~snm.x & 1u), (__float_as_uint(pa[1]) & ~1u) | (~snm.y & 1u),
                            (__float_as_uint(pa[2]) & ~1u) | (~snm.z & 1u), (__float_as_uint(pa[3]) & ~1u) | (~snm.w & 1u)});
      u32x4 a0 = {0u, 0u, 0u, 0u}, a1 = {0u, 0u, 0u, 0u};
      bool k0 = false, k1 = false;
      __builtin_amdgcn_s_sleep(8);
#pragma nounroll
      for (unsigned spin = 0; spin < GSPIN; ++spin) {
        const unsigned mask = lds_ld(&L.qrdy);
        WAIT_LKM0();
        const u32x4 n0 = ld_sc1(wsr, k0 ? OOB : o0), n1 = ld_sc1(wsr, k1 ? OOB : o1);
        WAIT_VM0();
        if (!k0) { a0 = n0; k0 = (lsb4(n0) ^ t0) == 15u || ((mask >> p) & 1u); }
        if (!k1) { a1 = n1; k1 = (lsb4(n1) ^ t1) == 15u || ((mask >> (p + 4)) & 1u); }
        if (__all(k0 && k1)) break;
        __builtin_amdgcn_s_sleep(2);
      }
      float q[4];
#pragma unroll
      for (int k = 0; k < 4; ++k) { float t = __uint_as_float(a0[k] & ~1u) + __uint_as_float(a1[k] & ~1u); t = tr16(t, t); q[k] = tr32(t, t); }
      if (p == 0) {
        bf16x2 lo = {(__bf16)q[0], (__bf16)q[1]}, hi = {(__bf16)q[2], (__bf16)q[3]};
        *(u32x2*)&L.qs[j * 32 + c4 * 2] = u32x2{__builtin_bit_cast(unsigned, lo), __builtin_bit_cast(unsigned, hi)};
      }
      CFENCE();
      if (lane == 0) lds_add(&L.qcnt);
      STAMP(2);
      if (TLX >= 0 && j == 0) load_tiles();
    } else {
      const int unit = j == 3 ? b : ((b & 1) ? -1 : 256 + (b >> 1));
      const unsigned u = unit < 0 ? 0 : unit, mat = u / 192, lb = (u % 192) >> 3, rp = u & 7;
      const unsigned tw = lane < 8 ? WS_TG + (h * 8 + lane) * TGS : (lane >= 32 && s == 0) ? WS_TG + (lane - 32) * 8 * TGS : OOB;
      unsigned snap = 0;
      if (j == 4) {
        const u32x4 snap2 = ld_sc1(wsr, tw);
        snap = snap2.x;
        const u32x4 sv = {SENT, SENT, SENT, SENT};
        st_sc1(wsr, WS_P1 + b * 1024 + lane * 16, sv);
        st_sc1(wsr, b < 160 ? WS_P2 + b * 2048 + lane * 16 : OOB, sv);
        st_sc1(wsr, b < 160 ? WS_P2 + b * 2048 + 1024 + lane * 16 : OOB, sv);
        const unsigned u2 = lane < 16 ? b : 256 + (b >> 1);
        const bool on = lane < 16 || (lane < 32 && !(b & 1));
        st_sc1(wsr, on ? WS_FO + (((u2 / 192) * 8 + (u2 & 7)) * 1536 + ((u2 % 192) >> 3) * 64 + (lane & 15) * 4) * 4 : OOB, sv);
        __builtin_amdgcn_sched_barrier(0);
        __builtin_amdgcn_s_barrier();
        WAIT_VM0();
        if (lane < 8) { L.tsn[0][lane] = snap2.y; L.tsn[1][lane] = snap2.z; }
        CFENCE();
        if (lane == 0) lds_add(&L.pre);
      } else {
        __builtin_amdgcn_s_barrier();
      }
      gemv_issue(g, xr, mkrsrc(mat ? w3 : w1, unit < 0 ? 0 : D * F * 2), F * 2, rp * 80, lb * 128, lane);
      if (TLX < 0) load_tiles();
      if (j == 4) {
        const unsigned long long tl = rt();
#pragma nounroll
        for (unsigned spin = 0; spin < LSPIN; ++spin) {
          if (rfl(lds_ld(&L.qcnt)) >= 3u) break;
          __builtin_amdgcn_s_sleep(2);
          if (rt() - tl > 800ull && (spin & 15u) == 0u) {
            const unsigned cur = ld4_sc1(wsr, lane < 8 ? tw : OOB);
            const unsigned qm = (unsigned)__ballot(lane < 8 && ((cur ^ snap) & 3u));
            if (lane == 0) lds_st(&L.qrdy, qm);
          }
        }
        CFENCE();
        st4_sc1(wsr, lane == s ? tw : OOB, snap ^ 1u);
        STAMP(1);
        const unsigned want = s == 0 ? 3u : 4u;
        bool seen = s == 0 ? lane < 32 : lane != 0;
#pragma nounroll
        for (unsigned spin = 0; spin < GSPIN; ++spin) {
          __builtin_amdgcn_s_sleep(16);
          const unsigned cur = ld4_sc1(wsr, seen ? OOB : tw);
          seen = seen || ((cur ^ snap) & want) != 0u;
          if (__all(seen)) break;
        }
        st4_sc1(wsr, (s == 0 && lane == 0) ? tw : OOB, snap ^ 7u);
        if (lane == 0) lds_st(&L.bdone, 1u);
        STAMP(2);
      }
      gemv_compute(g, pa, lane);
      st_sc1(wsr, (wr16 && unit >= 0) ? WS_FO + ((mat * 8 + rp) * 1536 + lb * 64 + col4) * 4 : OOB, u32x4{san(pa[0]), san(pa[1]), san(pa[2]), san(pa[3])});
    }
  }
  float gate;
  {
    const u32x4 gxa = __builtin_amdgcn_raw_buffer_load_b128(xr, lane * 20, 0, 0);
    const unsigned gxb = __builtin_amdgcn_raw_buffer_load_b32(xr, lane * 20 + 16, 0, 0);
    const rsrc_t gr = mkrsrc(gw, D * 2);
    const u32x4 gwa = __builtin_amdgcn_raw_buffer_load_b128(gr, lane * 20, 0, 0);
    const unsigned gwb = __builtin_amdgcn_raw_buffer_load_b32(gr, lane * 20 + 16, 0, 0);
    float z = 0.f;
#pragma unroll
    for (int k = 0; k < 4; ++k) { z = fmaf(bf_lo(gxa[k]), bf_lo(gwa[k]), z); z = fmaf(bf_hi(gxa[k]), bf_hi(gwa[k]), z); }
    z = fmaf(bf_lo(gxb), bf_lo(gwb), z); z = fmaf(bf_hi(gxb), bf_hi(gwb), z);
    z = sum_hi3(sum_lo3(z));
    gate = 1.f / (1.f + __expf(-z));
  }
  if (j == 2) {
    unsigned at = XCHK;
#pragma nounroll
    for (int st = 0; st < (NDYN2 > 0 ? 2 : 1); ++st) {
      const unsigned pool = st ? NDYN2 : NDYN;
      lds_wait_ge<4>(&L.tcons, at);
      const unsigned old = L.tsn[st][lane & 7];
      const unsigned ta = WS_TG + (h * 8 + (lane & 7)) * TGS + 4 + st * 4;
      st4_sc1(wsr, lane == s ? ta : OOB, ((unsigned)rt() << 1) | (~old & 1u));
      unsigned tv = 0;
      bool seen = lane >= 8;
#pragma nounroll
      for (unsigned spin = 0; spin < GSPIN; ++spin) {
        __builtin_amdgcn_s_sleep(8);
        const unsigned cur = ld4_sc1(wsr, seen ? OOB : ta);
        if (!seen) { tv = cur; seen = ((cur ^ old) & 1u) != 0u; }
        if (__all(seen)) break;
      }
      const int rel = (int)((tv & ~1u) - (rfl(tv) & ~1u)) >> 1;
      const float lag = (float)rel - sum_lo3((float)rel) * 0.125f;
      float n = fmaxf((float)pool * 0.125f - lag * RATE, 0.f);
      n *= (float)pool / sum_lo3(n);
      unsigned cum = (unsigned)(n * 16.f);
      { unsigned t = udpp<SHR1>(cum); cum += (lane & 7) >= 1 ? t : 0u; t = udpp<SHR2>(cum); cum += (lane & 7) >= 2 ? t : 0u; t = udpp<SHR4>(cum); cum += (lane & 7) >= 4 ? t : 0u; }
      const unsigned hi = (lane & 7) == 7 ? pool : min((cum + 8u) >> 4, pool);
      const unsigned hprev = udpp<SHR1>(hi);
      const unsigned lo = (lane & 7) == 0 ? 0u : hprev;
      if (lane == s) lds_st(st ? &L.tprod2 : &L.tprod, 0x80000000u | (lo << 12) | (hi - lo));
      at = NSW + __builtin_amdgcn_readlane(hi - lo, s) - R2;
    }
  }
  if (j == 3) {
    const int part = lane >> 4, mr = lane & 15;
    const unsigned o = part < 3 ? WS_FO + (mr * 1536 + grp * 12 + part * 4) * 4 : OOB;
    lds_wait_ge<8>(&L.bdone, 1u);
#pragma nounroll
    while (rt() - tstart < FFN_T && !rfl(lds_ld(&L.sflag[0]))) __builtin_amdgcn_s_sleep(32);
    u32x4 a;
#pragma nounroll
    for (unsigned spin = 0; spin < GSPIN; ++spin) {
      CFENCE();
      a = ld_sc1(wsr, o);
      if (__all(valid4(a))) break;
      __builtin_amdgcn_s_sleep(16);
    }
    float hv[4];
#pragma unroll
    for (int k = 0; k < 4; ++k) {
      const float t = sum_lo3(__uint_as_float(a[k]));
      const float o8 = dpp<XOR8>(t);
      const float gv = (mr & 8) ? o8 : t, uv = (mr & 8) ? t : o8;
      hv[k] = gv / (1.f + __expf(-gv)) * uv;
    }
    if (mr == 0) *(f32x4*)&L.hs[part * 4] = f32x4{hv[0], hv[1], hv[2], hv[3]};
    CFENCE();
    if (lane == 0) lds_st(&L.hrdy, 1u);
  }
  if (TLX >= 0 && j != 0) {
    if (TLX < 1000) {
      lds_wait_ge<8>(&L.tprod2, 0x80000000u);
      const unsigned tot = NSW + (rfl(lds_ld(&L.tprod)) & 0xfffu) + (rfl(lds_ld(&L.tprod2)) & 0xfffu);
      lds_wait_ge<1>(&L.tcons, tot + 4 - TLX);
    } else {
#pragma nounroll
      for (unsigned spin = 0; spin < LSPIN; ++spin) {
        const u32x4 f = *(volatile u32x4*)&L.sflag[0];
        if (rfl(f.x | f.y | f.z | f.w)) break;
        __builtin_amdgcn_s_sleep(2);
      }
      CFENCE();
    }
    load_tiles();
  }
  f32x2 wf[6][4];
#pragma unroll
  for (int t = 0; t < 6; ++t)
#pragma unroll
    for (int k = 0; k < 4; ++k) { wf[t][k] = f32x2{bf_lo(wot[t][k]), bf_hi(wot[t][k])}; asm volatile("" : "+v"(wf[t][k])); }
  lds_wait_ge<8>(&L.hrdy, 1u);
  f32x2 y2[4];
  {
    const float h0 = L.hs[rg] * (1.f - gate), h1 = L.hs[8 + rg] * (1.f - gate);
#pragma unroll
    for (int k = 0; k < 4; ++k) y2[k] = f32x2{h0 * bf_lo(w2t[0][k]) + h1 * bf_lo(w2t[1][k]), h0 * bf_hi(w2t[0][k]) + h1 * bf_hi(w2t[1][k])};
  }
  STAMP(3);
  if (j == 0) {
#pragma nounroll
    for (unsigned spin = 0; spin < LSPIN; ++spin) {
      const u32x4 f = *(volatile u32x4*)&L.sflag[0];
      if (rfl(f.x & f.y & f.z & f.w)) break;
      if (!rfl(f.x | f.y | f.z | f.w)) __builtin_amdgcn_s_sleep(2);
    }
    CFENCE();
    STAMP(4);
    {
      const int hf = lane & 1;
      f32x4 pv[NSTR]; f32x2 pm[NSTR];
#pragma unroll
      for (int q2 = 0; q2 < NSTR; ++q2) { pv[q2] = *(const f32x4*)&L.mrg[q2][lane * 4]; pm[q2] = *(const f32x2*)&L.ml[q2][0]; }
      float MM = pm[0].x;
#pragma unroll
      for (int q2 = 1; q2 < NSTR; ++q2) MM = fmaxf(MM, pm[q2].x);
      float o0 = 0.f, o1 = 0.f, o2 = 0.f, o3 = 0.f;
#pragma unroll
      for (int q2 = 0; q2 < NSTR; ++q2) {
        const float ww = __builtin_amdgcn_exp2f(pm[q2].x - MM);
        o0 = fmaf(ww, pv[q2].x, o0); o1 = fmaf(ww, pv[q2].y, o1);
        o2 = fmaf(ww, pv[q2].z, o2); o3 = fmaf(ww, hf ? pm[q2].y : pv[q2].w, o3);
      }
      if (hf) o2 = MM;
      st_sc1(wsr, WS_P1 + b * 1024 + lane * 16, u32x4{san(o0), san(o1), san(o2), san(o3)});
    }
    STAMP(7);
    const unsigned o = WS_P1 + (h * 8 + c) * 1024 + (rq * 8 + rg) * 32;
    u32x4 A = {0u, 0u, 0u, 0u}, B = {0u, 0u, 0u, 0u};
    bool ok = false;
#pragma nounroll
    for (unsigned spin = 0; spin < GSPIN; ++spin) {
      CFENCE();
      const u32x4 nA = ld_sc1(wsr, ok ? OOB : o), nB = ld_sc1(wsr, ok ? OOB : o + 16);
      if (!ok) { A = nA; B = nB; ok = valid4(nA) && valid4(nB); }
      if (__all(ok)) break;
    }
    STAMP(5);
    const float mp = __uint_as_float(B.z);
    const float M = max_lo3(mp);
    const float w = __builtin_amdgcn_exp2f(mp - M);
    const float sc = gate * __builtin_amdgcn_rcpf(sum_lo3(w * __uint_as_float(B.w)));
    const float o0 = sum_lo3(w * __uint_as_float(A.x)) * sc, o1 = sum_lo3(w * __uint_as_float(A.y)) * sc, o2 = sum_lo3(w * __uint_as_float(A.z)) * sc;
    const float o3 = sum_lo3(w * __uint_as_float(A.w)) * sc, o4 = sum_lo3(w * __uint_as_float(B.x)) * sc, o5 = sum_lo3(w * __uint_as_float(B.y)) * sc;
    if (c == 0) { *(f32x4*)&L.os[rg * 8] = f32x4{o0, o1, o2, o3}; *(f32x2*)&L.os[rg * 8 + 4] = f32x2{o4, o5}; }
    CFENCE();
    if (lane == 0) lds_st(&L.oflag, 1u);
    CFENCE();
  } else {
    lds_wait_ge<4>(&L.sflag[0], 1u);
    lds_wait_ge<0>(&L.oflag, 1u);
  }
  {
    const f32x4 oa = *(const f32x4*)&L.os[rg * 8];
    const f32x2 ob = *(const f32x2*)&L.os[rg * 8 + 4];
#pragma unroll
    for (int t = 0; t < 6; ++t) {
      const float ov = t < 4 ? oa[t] : ob[t - 4];
#pragma unroll
      for (int k = 0; k < 4; ++k) y2[k] = __builtin_elementwise_fma(f32x2{ov, ov}, wf[t][k], y2[k]);
    }
    float r4[4];
#pragma unroll
    for (int k = 0; k < 4; ++k) { float t = tr32(y2[k >> 1][k & 1], y2[2 + (k >> 1)][k & 1]); t = tr16(t, t); r4[k] = t + dpp<XOR8>(t); }
    st_sc1(wsr, wr16 ? WS_P2 + (half * 80 + j * 16 + (col4 >> 2)) * 2048 + grp * 16 : OOB, u32x4{san(r4[0]), san(r4[1]), san(r4[2]), san(r4[3])});
  }
  STAMP(6);
  if (j == 1 && b < 160) {
    const unsigned o0 = WS_P2 + b * 2048 + lane * 16;
    u32x4 a0 = {0u, 0u, 0u, 0u}, a1 = {0u, 0u, 0u, 0u};
    bool k0 = false, k1 = false;
#pragma nounroll
    for (unsigned spin = 0; spin < GSPIN; ++spin) {
      CFENCE();
      const u32x4 n0 = ld_sc1(wsr, k0 ? OOB : o0), n1 = ld_sc1(wsr, k1 ? OOB : o0 + 1024);
      if (!k0) { a0 = n0; k0 = valid4(n0); }
      if (!k1) { a1 = n1; k1 = valid4(n1); }
      if (__all(k0 && k1)) break;
    }
    const float v0 = __uint_as_float(a0.x) + __uint_as_float(a1.x), v1 = __uint_as_float(a0.y) + __uint_as_float(a1.y);
    const float v2 = __uint_as_float(a0.z) + __uint_as_float(a1.z), v3 = __uint_as_float(a0.w) + __uint_as_float(a1.w);
    float t = tr16(tr32(v0, v2), tr32(v1, v3));
    t += dpp<XOR8>(t); t = sum_lo3(t);
    const __bf16 ov = (__bf16)t;
    __builtin_amdgcn_raw_buffer_store_b16(__builtin_bit_cast(unsigned short, ov), mkrsrc(out, D * 2), (lane & 15) == 0 ? (b * 4 + (lane >> 4)) * 2 : OOB, 0, 0);
    STAMP(7);
  }
  STAMP_FLUSH();
}

extern "C" void kernel_launch(void* const* d_in, const int* in_sizes, int n_in, void* d_out, int out_size, void* d_ws, size_t ws_size,
                              hipStream_t stream) {
  fused_kernel<<<256, NTHR, 0, stream>>>((const uint16_t*)d_in[0], (const uint16_t*)d_in[1], (const uint16_t*)d_in[2], (const uint16_t*)d_in[3],
                                         (const uint16_t*)d_in[4], (const uint16_t*)d_in[6], (const uint16_t*)d_in[7], (const uint16_t*)d_in[8],
                                         (const uint16_t*)d_in[9], (unsigned char*)d_ws, (uint16_t*)d_out);
}
```

```cpp
#include <hip/hip_runtime.h>
#include <hip/hip_bf16.h>
#include <stdint.h>
#include <stddef.h>

typedef unsigned u32x4 __attribute__((ext_vector_type(4)));
typedef unsigned u32x2 __attribute__((ext_vector_type(2)));
typedef float f32x4 __attribute__((ext_vector_type(4)));
typedef float f32x2 __attribute__((ext_vector_type(2)));
typedef __bf16 bf16x2 __attribute__((ext_vector_type(2)));
typedef __amdgpu_buffer_rsrc_t rsrc_t;

constexpr int RING = 4;
constexpr int AHEAD = 1;
constexpr int PREW = 3;
constexpr int PMAX = 6;
constexpr unsigned NDYN = 128, NDYN2 = 64;
constexpr unsigned XCHK = 28;
constexpr unsigned R2 = 16;
constexpr float RATE = 0.05f;
constexpr unsigned FFN_T = 2000;

constexpr int D = 640, H = 32, SK = 8192, DH = 192, F = 1536;
constexpr int NHELP = 5, NSTR = 4, NWAVES = NHELP + NSTR, NTHR = NWAVES * 64;
constexpr int AUX_NT = 2, AUX_SC1 = 16;
constexpr unsigned OOB = 0x7ffffff0u;
constexpr unsigned WS_QO = 0;
constexpr unsigned WS_FO = WS_QO + 256 * 192 * 4;
constexpr unsigned WS_P1 = WS_FO + 16 * 1536 * 4;
constexpr unsigned WS_P2 = WS_P1 + 256 * 1024;
constexpr unsigned WS_TG = WS_P2 + 160 * 2048;
constexpr unsigned TGS = 128;
constexpr unsigned WS_END = WS_TG + 256 * TGS;
constexpr unsigned WS_STAMP = 2u << 20;
constexpr unsigned SENT = 0xFFFFFFFFu;
constexpr unsigned NWH = 8 * NSTR, STAT = 1024 - NDYN - NDYN2, UPW = STAT / 8;
constexpr unsigned R0 = RING + PMAX + AHEAD;
constexpr unsigned NSW = UPW - R0 * NSTR;
static_assert(STAT % NWH == 0 && UPW >= R0 * NSTR && AHEAD < RING && XCHK <= NSW && R2 <= NSW - XCHK, "work split");

__device__ __forceinline__ float bf_lo(unsigned u) { return __builtin_bit_cast(float, u << 16); }
__device__ __forceinline__ float bf_hi(unsigned u) { return __builtin_bit_cast(float, u & 0xffff0000u); }
__device__ __forceinline__ float dot2(unsigned k, unsigned q, float acc) {
  return __builtin_amdgcn_fdot2_f32_bf16(__builtin_bit_cast(bf16x2, k), __builtin_bit_cast(bf16x2, q), acc, false);
}
template <int CTRL> __device__ __forceinline__ float dpp(float x) {
  return __builtin_bit_cast(float, __builtin_amdgcn_mov_dpp(__builtin_bit_cast(int, x), CTRL, 0xf, 0xf, true));
}
template <int CTRL> __device__ __forceinline__ unsigned udpp(unsigned x) { return (unsigned)__builtin_amdgcn_mov_dpp((int)x, CTRL, 0xf, 0xf, true); }
constexpr int XOR1 = 0xB1, XOR2 = 0x4E, HMIR = 0x141, XOR8 = 0x128, SHR1 = 0x111, SHR2 = 0x112, SHR4 = 0x114;
__device__ __forceinline__ float tr16(float a, float b) {
  auto s = __builtin_amdgcn_permlane16_swap(__float_as_uint(a), __float_as_uint(b), false, false);
  return __uint_as_float(s[0]) + __uint_as_float(s[1]);
}
__device__ __forceinline__ float tr32(float a, float b) {
  auto s = __builtin_amdgcn_permlane32_swap(__float_as_uint(a), __float_as_uint(b), false, false);
  return __uint_as_float(s[0]) + __uint_as_float(s[1]);
}
__device__ __forceinline__ float max16(float x) {
  auto s = __builtin_amdgcn_permlane16_swap(__float_as_uint(x), __float_as_uint(x), false, false);
  return fmaxf(__uint_as_float(s[0]), __uint_as_float(s[1]));
}
__device__ __forceinline__ float max32(float x) {
  auto s = __builtin_amdgcn_permlane32_swap(__float_as_uint(x), __float_as_uint(x), false, false);
  return fmaxf(__uint_as_float(s[0]), __uint_as_float(s[1]));
}
__device__ __forceinline__ float sum_hi3(float x) { x += dpp<XOR8>(x); x = tr16(x, x); return tr32(x, x); }
__device__ __forceinline__ float sum_lo3(float x) { x += dpp<XOR1>(x); x += dpp<XOR2>(x); x += dpp<HMIR>(x); return x; }
__device__ __forceinline__ float max_lo3(float x) { x = fmaxf(x, dpp<XOR1>(x)); x = fmaxf(x, dpp<XOR2>(x)); return fmaxf(x, dpp<HMIR>(x)); }
__device__ __forceinline__ unsigned san(float v) { unsigned u = __float_as_uint(v); return u < 0xFFFFFFFEu ? u : 0xFFFFFFFEu; }
__device__ __forceinline__ rsrc_t mkrsrc(const void* p, unsigned bytes) { return __builtin_amdgcn_make_buffer_rsrc((void*)p, 0, bytes, 0x00020000); }
__device__ __forceinline__ u32x4 ld_nt(rsrc_t r, unsigned voff) { return __builtin_amdgcn_raw_buffer_load_b128(r, voff, 0, AUX_NT); }
__device__ __forceinline__ u32x4 ld_sc1(rsrc_t r, unsigned voff) { return __builtin_amdgcn_raw_buffer_load_b128(r, voff, 0, AUX_SC1); }
__device__ __forceinline__ unsigned ld4_sc1(rsrc_t r, unsigned voff) { return __builtin_amdgcn_raw_buffer_load_b32(r, voff, 0, AUX_SC1); }
__device__ __forceinline__ void st_sc1(rsrc_t r, unsigned voff, u32x4 v) { __builtin_amdgcn_raw_buffer_store_b128(v, r, voff, 0, AUX_SC1); }
__device__ __forceinline__ void st4_sc1(rsrc_t r, unsigned voff, unsigned v) { __builtin_amdgcn_raw_buffer_store_b32(v, r, voff, 0, AUX_SC1); }
__device__ __forceinline__ bool valid4(u32x4 v) { return v.x != SENT && v.y != SENT && v.z != SENT && v.w != SENT; }
__device__ __forceinline__ unsigned lsb4(u32x4 v) { return (v.x & 1u) | ((v.y & 1u) << 1) | ((v.z & 1u) << 2) | ((v.w & 1u) << 3); }
__device__ __forceinline__ unsigned long long rt() { return __builtin_amdgcn_s_memrealtime(); }
__device__ __forceinline__ unsigned lds_ld(const unsigned* p) { return __hip_atomic_load(p, __ATOMIC_RELAXED, __HIP_MEMORY_SCOPE_WORKGROUP); }
__device__ __forceinline__ void lds_st(unsigned* p, unsigned v) { __hip_atomic_store(p, v, __ATOMIC_RELAXED, __HIP_MEMORY_SCOPE_WORKGROUP); }
__device__ __forceinline__ unsigned lds_add(unsigned* p) { return __hip_atomic_fetch_add(p, 1u, __ATOMIC_RELAXED, __HIP_MEMORY_SCOPE_WORKGROUP); }
__device__ __forceinline__ unsigned rfl(unsigned v) { return __builtin_amdgcn_readfirstlane(v); }
constexpr unsigned GSPIN = 1u << 15;
constexpr unsigned LSPIN = 1u << 19;
template <int SLEEP> __device__ __forceinline__ void lds_wait_ge(const unsigned* p, unsigned v) {
#pragma nounroll
  for (unsigned spin = 0; spin < LSPIN; ++spin) {
    if (rfl(lds_ld(p)) >= v) break;
    __builtin_amdgcn_s_sleep(SLEEP);
  }
  asm volatile("" ::: "memory");
}
#define WAIT_VM0() asm volatile("s_waitcnt vmcnt(0)" ::: "memory")
#define WAIT_LKM0() asm volatile("s_waitcnt lgkmcnt(0)" ::: "memory")
#define CFENCE() asm volatile("" ::: "memory")

#define STAMP(i) do {} while (0)
#define STAMP_FLUSH() do {} while (0)

template <int N> struct IC { static constexpr int value = N; };
template <int B, int E, class Fn> __device__ __forceinline__ void static_for(Fn&& f) { if constexpr (B < E) { f(IC<B>{}); static_for<B + 1, E>(f); } }

struct Lds {
  float mrg[NSTR][256];
  float ml[NSTR][2];
  unsigned qs[96];
  float os[64];
  float hs[16];
  unsigned qcnt;
  unsigned pre;
  unsigned qrdy;
  unsigned bdone;
  unsigned hrdy;
  unsigned oflag;
  unsigned tcons;
  unsigned tprod;
  unsigned sflag[4];
  unsigned tsn[2][8];
  unsigned tprod2, pad2[3];
  u32x4 park[NSTR][PMAX][6][64];
};
static_assert(offsetof(Lds, sflag) % 16 == 0 && offsetof(Lds, os) % 16 == 0 && offsetof(Lds, qs) % 16 == 0 && offsetof(Lds, hs) % 16 == 0 && offsetof(Lds, park) % 16 == 0, "LDS alignment");
static_assert(sizeof(Lds) <= 160 * 1024, "LDS size");

struct GemvLoads { u32x4 w[10]; unsigned xs; };
__device__ __forceinline__ void gemv_issue(GemvLoads& g, rsrc_t xr, rsrc_t wr, unsigned ldB, unsigned row0, unsigned colB, int lane) {
  const unsigned off = (row0 + (lane >> 3)) * ldB + colB + (lane & 7) * 16;
#pragma unroll
  for (int i = 0; i < 10; ++i) g.w[i] = ld_nt(wr, off + i * 8 * ldB);
  g.xs = __builtin_amdgcn_raw_buffer_load_b32(xr, lane < 40 ? row0 * 2 + lane * 4 : OOB, 0, 0);
}
__device__ __forceinline__ void gemv_compute(const GemvLoads& g, float (&r)[4], int lane) {
  float acc[8];
#pragma unroll
  for (int k = 0; k < 8; ++k) acc[k] = 0.f;
  const int rg = lane >> 3;
#pragma unroll
  for (int i = 0; i < 10; ++i) {
    const unsigned xw = (unsigned)__builtin_amdgcn_ds_bpermute((i * 4 + (rg >> 1)) * 4, (int)g.xs);
    const float xv = (rg & 1) ? bf_hi(xw) : bf_lo(xw);
#pragma unroll
    for (int k = 0; k < 4; ++k) { acc[2 * k] = fmaf(xv, bf_lo(g.w[i][k]), acc[2 * k]); acc[2 * k + 1] = fmaf(xv, bf_hi(g.w[i][k]), acc[2 * k + 1]); }
  }
#pragma unroll
  for (int k = 0; k < 4; ++k) { float t = tr32(acc[k], acc[k + 4]); t = tr16(t, t); r[k] = t + dpp<XOR8>(t); }
}

__global__ void __launch_bounds__(NTHR) fused_kernel(const uint16_t* __restrict__ x, const uint16_t* __restrict__ kc, const uint16_t* __restrict__ vc,
                                                     const uint16_t* __restrict__ wq, const uint16_t* __restrict__ wo, const uint16_t* __restrict__ w1,
                                                     const uint16_t* __restrict__ w3, const uint16_t* __restrict__ w2, const uint16_t* __restrict__ gw,
                                                     unsigned char* __restrict__ ws, uint16_t* __restrict__ out) {
  __shared__ __attribute__((aligned(16))) Lds L;
  const int b = blockIdx.x, h = b >> 3, s = b & 7;
  const int tid = threadIdx.x, lane = tid & 63, wave = __builtin_amdgcn_readfirstlane(tid >> 6);
  const int rg = lane >> 3, c = lane & 7;
  STAMP(0);
  asm volatile("" :: "s"(x), "s"(kc), "s"(vc), "s"(wq), "s"(wo), "s"(w1), "s"(w3), "s"(w2), "s"(gw), "s"(ws), "s"(out));
  const rsrc_t wsr = mkrsrc(ws, WS_END);

  if (wave >= NHELP) {
    const int sw = wave - NHELP;
    const unsigned long long kb = (unsigned long long)(kc + (size_t)h * SK * DH), vb = (unsigned long long)(vc + (size_t)h * SK * DH);
    const u32x4 kr = {(unsigned)kb, (unsigned)(kb >> 32) & 0xffffu, (unsigned)(SK * DH * 2), 0x00020000u};
    const u32x4 vr = {(unsigned)vb, (unsigned)(vb >> 32) & 0xffffu, (unsigned)(SK * DH * 2), 0x00020000u};
    const unsigned loff = (unsigned)(rg * DH + c * 8) * 2;
    constexpr unsigned STEPB = 8 * DH * 2;
    u32x4 K[RING][3], V[RING][3];
#define RING_LOADS "s_nop 4\n\t" \
                   "buffer_load_dwordx4 %0, %6, %7, %9 offen nt\n\t" \
                   "buffer_load_dwordx4 %1, %6, %7, %9 offen offset:128 nt\n\t" \
                   "buffer_load_dwordx4 %2, %6, %7, %9 offen offset:256 nt\n\t" \
                   "buffer_load_dwordx4 %3, %6, %8, %9 offen nt\n\t" \
                   "buffer_load_dwordx4 %4, %6, %8, %9 offen offset:128 nt\n\t" \
                   "buffer_load_dwordx4 %5, %6, %8, %9 offen offset:256 nt"
    auto issue0 = [&](int slot, unsigned unit) {
      const unsigned so = unit * STEPB;
      asm volatile(RING_LOADS
                   : "=&v"(K[slot][0]), "=&v"(K[slot][1]), "=&v"(K[slot][2]), "=&v"(V[slot][0]), "=&v"(V[slot][1]), "=&v"(V[slot][2])
                   : "v"(loff), "s"(kr), "s"(vr), "s"(so));
    };
    auto issue = [&](int slot, unsigned unit) {
      const unsigned so = unit * STEPB;
      asm volatile(RING_LOADS
                   : "+v"(K[slot][0]), "+v"(K[slot][1]), "+v"(K[slot][2]), "+v"(V[slot][0]), "+v"(V[slot][1]), "+v"(V[slot][2])
                   : "v"(loff), "s"(kr), "s"(vr), "s"(so));
    };
#pragma unroll
    for (int i = 0; i < RING; ++i) {
      if (i >= 1) asm volatile("s_waitcnt vmcnt(%0)" :: "i"(PREW) : "memory");
      issue0(i, i * NWH + s * NSTR + sw);
      __builtin_amdgcn_sched_barrier(0);
      if (i == 0) { __builtin_amdgcn_s_barrier(); __builtin_amdgcn_sched_barrier(0); }
    }
    unsigned pi = RING;
    auto private_unit = [&]() -> unsigned { return (pi++) * NWH + s * NSTR + sw; };
    int np = 0;
    {
      const unsigned pbase = rfl((unsigned)(size_t)&L.park[sw][0][0][0]);
#pragma nounroll
      while (np < PMAX) {
        asm volatile("s_waitcnt vmcnt(%0)" :: "i"(PREW) : "memory");
        if (rfl(lds_ld(&L.qcnt)) >= 3u) break;
        const unsigned so = private_unit() * STEPB, mb = pbase + np * 6144;
        unsigned keep;
        asm volatile("s_mov_b32 %0, m0\n\t"
                     "s_mov_b32 m0, %1\n\ts_nop 4\n\t"
                     "buffer_load_dwordx4 %2, %3, %5 offen nt lds\n\t"
                     "s_add_u32 m0, %1, 896\n\ts_nop 0\n\t"
                     "buffer_load_dwordx4 %2, %3, %5 offen offset:128 nt lds\n\t"
                     "s_add_u32 m0, %1, 1792\n\ts_nop 0\n\t"
                     "buffer_load_dwordx4 %2, %3, %5 offen offset:256 nt lds\n\t"
                     "s_add_u32 m0, %1, 3072\n\ts_nop 0\n\t"
                     "buffer_load_dwordx4 %2, %4, %5 offen nt lds\n\t"
                     "s_add_u32 m0, %1, 3968\n\ts_nop 0\n\t"
                     "buffer_load_dwordx4 %2, %4, %5 offen offset:128 nt lds\n\t"
                     "s_add_u32 m0, %1, 4864\n\ts_nop 0\n\t"
                     "buffer_load_dwordx4 %2, %4, %5 offen offset:256 nt lds\n\t"
                     "s_mov_b32 m0, %0"
                     : "=&s"(keep) : "s"(mb), "v"(loff), "s"(kr), "s"(vr), "s"(so) : "scc", "memory");
        ++np;
      }
    }
    STAMP(1);
    constexpr float C2 = 0.07216878364870322f * 1.4426950408889634f;
    float m = -INFINITY, l = 0.f, p_, alpha_;
    f32x2 acc2[12];
#pragma unroll
    for (int i = 0; i < 12; ++i) acc2[i] = f32x2{0.f, 0.f};
    u32x4 qp[3];
    auto scores = [&](const u32x4 (&Kx)[3]) {
      float d3[3];
#pragma unroll
      for (int jj = 0; jj < 3; ++jj) {
        d3[jj] = 0.f;
#pragma unroll
        for (int k = 0; k < 4; ++k) d3[jj] = dot2(Kx[jj][k], qp[jj][k], d3[jj]);
      }
      const float sc = sum_lo3(d3[0] + d3[1] + d3[2]) * C2;
      float pm = fmaxf(sc, dpp<XOR8>(sc)); pm = max32(max16(pm));
      const float mn = fmaxf(m, pm);
      alpha_ = __builtin_amdgcn_exp2f(m - mn); p_ = __builtin_amdgcn_exp2f(sc - mn);
      l = fmaf(l, alpha_, p_); m = mn;
    };
    auto accum = [&](const u32x4 (&Vx)[3]) {
      if (rfl(__float_as_uint(alpha_)) != 0x3f800000u) {
#pragma unroll
        for (int i = 0; i < 12; ++i) acc2[i] *= f32x2{alpha_, alpha_};
      }
#pragma unroll
      for (int jj = 0; jj < 3; ++jj)
#pragma unroll
        for (int k = 0; k < 4; ++k) acc2[jj * 4 + k] = __builtin_elementwise_fma(f32x2{p_, p_}, f32x2{bf_lo(Vx[jj][k]), bf_hi(Vx[jj][k])}, acc2[jj * 4 + k]);
#pragma unroll
      for (int jj = 0; jj < 3; ++jj) asm volatile("" : "+v"(acc2[jj * 4]), "+v"(acc2[jj * 4 + 1]), "+v"(acc2[jj * 4 + 2]), "+v"(acc2[jj * 4 + 3]));
    };
    auto consume = [&](int slot, auto younger) {
      constexpr int YN = decltype(younger)::value * 6;
      asm volatile("s_waitcnt vmcnt(%3)" : "+v"(K[slot][0]), "+v"(K[slot][1]), "+v"(K[slot][2]) : "i"(YN + 3));
      scores(K[slot]);
      asm volatile("s_waitcnt vmcnt(%3)" : "+v"(V[slot][0]), "+v"(V[slot][1]), "+v"(V[slot][2]) : "i"(YN));
      accum(V[slot]);
    };
    auto consume_parked = [&](int ip) {
      u32x4 T[3];
#pragma unroll
      for (int jj = 0; jj < 3; ++jj) T[jj] = L.park[sw][ip][jj][lane];
      scores(T);
      __builtin_amdgcn_sched_barrier(0);
#pragma unroll
      for (int jj = 0; jj < 3; ++jj) T[jj] = L.park[sw][ip][3 + jj][lane];
      accum(T);
    };
    lds_wait_ge<1>(&L.qcnt, 3u);
#pragma unroll
    for (int jj = 0; jj < 3; ++jj) qp[jj] = *(const u32x4*)&L.qs[jj * 32 + c * 4];
    STAMP(2);
    static_for<0, RING>([&](auto ic) {
      constexpr int i = decltype(ic)::value;
      consume(i, IC<RING - 1 - i + (i < AHEAD ? i : AHEAD)>{});
      if constexpr (i < AHEAD) issue(i, private_unit());
      __builtin_amdgcn_sched_barrier(0);
    });
    int rem = AHEAD;
    bool more = true;
#pragma nounroll
    do {
#pragma unroll
      for (int k = 0; k < RING; ++k) {
        if (rem > 0) {
          unsigned un = 1024u;
          if (pi < R0) un = private_unit();
          else if (more) {
            unsigned idx = 0;
            if (lane == 0) idx = lds_add(&L.tcons);
            idx = rfl(idx);
            if (idx < NSW) un = (R0 + idx / NSTR) * NWH + s * NSTR + idx % NSTR;
            else {
              idx -= NSW;
#pragma nounroll
              for (unsigned spin = 0; spin < LSPIN; ++spin) {
                const unsigned pr = rfl(lds_ld(&L.tprod));
                if (pr >> 31) {
                  if (idx < (pr & 0xfffu)) { un = STAT + ((pr >> 12) & 0xfffu) + idx; break; }
                  const unsigned p2 = rfl(lds_ld(&L.tprod2)), i2 = idx - (pr & 0xfffu);
                  if (p2 >> 31) { if (i2 < (p2 & 0xfffu)) un = STAT + NDYN + ((p2 >> 12) & 0xfffu) + i2; break; }
                }
                __builtin_amdgcn_s_sleep(1);
              }
            }
          }
          more = un < 1024u;
          issue((k + AHEAD) % RING, un);
          rem += more ? 0 : -1;
          consume(k, IC<AHEAD>{});
          __builtin_amdgcn_sched_barrier(0);
        }
      }
      if (np > 0) { CFENCE(); --np; consume_parked(np); __builtin_amdgcn_sched_barrier(0); }
    } while (rem > 0 || np > 0);
#pragma unroll
    for (int r2 = 0; r2 < RING; ++r2)
      asm volatile("s_waitcnt vmcnt(0)" :: "v"(K[r2][0]), "v"(K[r2][1]), "v"(K[r2][2]), "v"(V[r2][0]), "v"(V[r2][1]), "v"(V[r2][2]));
    __builtin_amdgcn_sched_barrier(0);
    STAMP(3);
    float r6[6];
    {
      float r12[12];
#pragma unroll
      for (int i = 0; i < 12; ++i) r12[i] = tr32(acc2[i >> 1][i & 1], acc2[6 + (i >> 1)][i & 1]);
#pragma unroll
      for (int i = 0; i < 6; ++i) { const float t = tr16(r12[i], r12[i + 6]); r6[i] = t + dpp<XOR8>(t); }
    }
    const float lw = sum_hi3(l);
    if ((lane & 8) == 0) {
      const int slot = c * 4 + (lane >> 4);
      *(f32x4*)&L.mrg[sw][slot * 8] = f32x4{r6[0], r6[1], r6[2], r6[3]};
      *(f32x2*)&L.mrg[sw][slot * 8 + 4] = f32x2{r6[4], r6[5]};
    } else if (lane == 8) *(f32x2*)&L.ml[sw][0] = f32x2{m, lw};
    CFENCE();
    if (lane == 0) lds_st(&L.sflag[sw], 1u);
    STAMP(4);
    STAMP_FLUSH();
    return;
  }

  const unsigned long long tstart = rt();
  const rsrc_t xr = mkrsrc(x, D * 2);
  const int j = wave;
  const int rq = s >> 1, half = s & 1;
  const bool wr16 = (lane & 0x18) == 0;
  const unsigned col4 = c * 8 + (lane >> 5) * 4;
  const unsigned grp = h * 4 + rq;
  u32x4 wot[6], w2t[2];
#pragma unroll
  for (int t = 0; t < 6; ++t) wot[t] = u32x4{0u, 0u, 0u, 0u};
  w2t[0] = w2t[1] = u32x4{0u, 0u, 0u, 0u};
  auto load_tiles = [&]() {
    const unsigned colB = (half * 320 + j * 64 + c * 8) * 2;
    const rsrc_t wor = mkrsrc(wo, H * DH * D * 2);
    const int cs = rq * 2 + (rg >> 2), i0 = (rg & 3) * 6;
#pragma unroll
    for (int t = 0; t < 6; ++t) { const int i = i0 + t; const unsigned d = (i >> 3) * 64 + cs * 8 + (i & 7); wot[t] = ld_nt(wor, (h * DH + d) * (D * 2) + colB); }
    const rsrc_t w2r = mkrsrc(w2, F * D * 2);
    w2t[0] = ld_nt(w2r, (grp * 12 + rg) * (D * 2) + colB);
    w2t[1] = ld_nt(w2r, rg < 4 ? (grp * 12 + 8 + rg) * (D * 2) + colB : OOB);
  };
  {
    GemvLoads g;
    float pa[4];
    if (j < 3) {
      const int p = lane >> 4, c4 = lane & 15;
      const unsigned o0 = WS_QO + ((h * 8 + p) * 192 + j * 64 + c4 * 4) * 4, o1 = o0 + 4 * 192 * 4;
      const unsigned om = wr16 ? WS_QO + (b * 192 + j * 64 + col4) * 4 : OOB;
      const u32x4 sn0 = ld_sc1(wsr, o0), sn1 = ld_sc1(wsr, o1), snm = ld_sc1(wsr, om);
      gemv_issue(g, xr, mkrsrc(wq, D * H * DH * 2), H * DH * 2, s * 80, (h * DH + j * 64) * 2, lane);
      __builtin_amdgcn_sched_barrier(0);
      if (tid < 12) (&L.qcnt)[tid] = 0u;
      if (tid == 12) L.tprod2 = 0u;
      WAIT_LKM0();
      __builtin_amdgcn_s_barrier();
      __builtin_amdgcn_sched_barrier(0);
      const unsigned t0 = lsb4(sn0), t1 = lsb4(sn1);
      asm volatile("" :: "v"(t0), "v"(t1), "v"(snm.x) : "memory");
      if (lane == 0) lds_add(&L.pre);
      gemv_compute(g, pa, lane);
      STAMP(1);
      asm volatile("" :: "v"(pa[0]), "v"(pa[1]), "v"(pa[2]), "v"(pa[3]) : "memory");
      lds_wait_ge<0>(&L.pre, 4u);
      st_sc1(wsr, om, u32x4{(__float_as_uint(pa[0]) & ~1u) | (~snm.x & 1u), (__float_as_uint(pa[1]) & ~1u) | (~snm.y & 1u),
                            (__float_as_uint(pa[2]) & ~1u) | (~snm.z & 1u), (__float_as_uint(pa[3]) & ~1u) | (~snm.w & 1u)});
      u32x4 a0 = {0u, 0u, 0u, 0u}, a1 = {0u, 0u, 0u, 0u};
      bool k0 = false, k1 = false;
      __builtin_amdgcn_s_sleep(8);
#pragma nounroll
      for (unsigned spin = 0; spin < GSPIN; ++spin) {
        const unsigned mask = lds_ld(&L.qrdy);
        WAIT_LKM0();
        const u32x4 n0 = ld_sc1(wsr, k0 ? OOB : o0), n1 = ld_sc1(wsr, k1 ? OOB : o1);
        WAIT_VM0();
        if (!k0) { a0 = n0; k0 = (lsb4(n0) ^ t0) == 15u || ((mask >> p) & 1u); }
        if (!k1) { a1 = n1; k1 = (lsb4(n1) ^ t1) == 15u || ((mask >> (p + 4)) & 1u); }
        if (__all(k0 && k1)) break;
        __builtin_amdgcn_s_sleep(2);
      }
      float q[4];
#pragma unroll
      for (int k = 0; k < 4; ++k) { float t = __uint_as_float(a0[k] & ~1u) + __uint_as_float(a1[k] & ~1u); t = tr16(t, t); q[k] = tr32(t, t); }
      if (p == 0) {
        bf16x2 lo = {(__bf16)q[0], (__bf16)q[1]}, hi = {(__bf16)q[2], (__bf16)q[3]};
        *(u32x2*)&L.qs[j * 32 + c4 * 2] = u32x2{__builtin_bit_cast(unsigned, lo), __builtin_bit_cast(unsigned, hi)};
      }
      CFENCE();
      if (lane == 0) lds_add(&L.qcnt);
      STAMP(2);
      if (j == 0) load_tiles();
    } else {
      const int unit = j == 3 ? b : ((b & 1) ? -1 : 256 + (b >> 1));
      const unsigned u = unit < 0 ? 0 : unit, mat = u / 192, lb = (u % 192) >> 3, rp = u & 7;
      const unsigned tw = lane < 8 ? WS_TG + (h * 8 + lane) * TGS : (lane >= 32 && s == 0) ? WS_TG + (lane - 32) * 8 * TGS : OOB;
      unsigned snap = 0;
      if (j == 4) {
        const u32x4 snap2 = ld_sc1(wsr, tw);
        snap = snap2.x;
        const u32x4 sv = {SENT, SENT, SENT, SENT};
        st_sc1(wsr, WS_P1 + b * 1024 + lane * 16, sv);
        st_sc1(wsr, b < 160 ? WS_P2 + b * 2048 + lane * 16 : OOB, sv);
        st_sc1(wsr, b < 160 ? WS_P2 + b * 2048 + 1024 + lane * 16 : OOB, sv);
        const unsigned u2 = lane < 16 ? b : 256 + (b >> 1);
        const bool on = lane < 16 || (lane < 32 && !(b & 1));
        st_sc1(wsr, on ? WS_FO + (((u2 / 192) * 8 + (u2 & 7)) * 1536 + ((u2 % 192) >> 3) * 64 + (lane & 15) * 4) * 4 : OOB, sv);
        __builtin_amdgcn_sched_barrier(0);
        __builtin_amdgcn_s_barrier();
        WAIT_VM0();
        if (lane < 8) { L.tsn[0][lane] = snap2.y; L.tsn[1][lane] = snap2.z; }
        CFENCE();
        if (lane == 0) lds_add(&L.pre);
      } else {
        __builtin_amdgcn_s_barrier();
      }
      gemv_issue(g, xr, mkrsrc(mat ? w3 : w1, unit < 0 ? 0 : D * F * 2), F * 2, rp * 80, lb * 128, lane);
      if (j == 4) {
        const unsigned long long tl = rt();
#pragma nounroll
        for (unsigned spin = 0; spin < LSPIN; ++spin) {
          if (rfl(lds_ld(&L.qcnt)) >= 3u) break;
          __builtin_amdgcn_s_sleep(2);
          if (rt() - tl > 800ull && (spin & 15u) == 0u) {
            const unsigned cur = ld4_sc1(wsr, lane < 8 ? tw : OOB);
            const unsigned qm = (unsigned)__ballot(lane < 8 && ((cur ^ snap) & 3u));
            if (lane == 0) lds_st(&L.qrdy, qm);
          }
        }
        CFENCE();
        st4_sc1(wsr, lane == s ? tw : OOB, snap ^ 1u);
        STAMP(1);
        const unsigned want = s == 0 ? 3u : 4u;
        bool seen = s == 0 ? lane < 32 : lane != 0;
#pragma nounroll
        for (unsigned spin = 0; spin < GSPIN; ++spin) {
          __builtin_amdgcn_s_sleep(16);
          const unsigned cur = ld4_sc1(wsr, seen ? OOB : tw);
          seen = seen || ((cur ^ snap) & want) != 0u;
          if (__all(seen)) break;
        }
        st4_sc1(wsr, (s == 0 && lane == 0) ? tw : OOB, snap ^ 7u);
        if (lane == 0) lds_st(&L.bdone, 1u);
        STAMP(2);
      }
      gemv_compute(g, pa, lane);
      st_sc1(wsr, (wr16 && unit >= 0) ? WS_FO + ((mat * 8 + rp) * 1536 + lb * 64 + col4) * 4 : OOB, u32x4{san(pa[0]), san(pa[1]), san(pa[2]), san(pa[3])});
    }
  }
  float gate;
  {
    const u32x4 gxa = __builtin_amdgcn_raw_buffer_load_b128(xr, lane * 20, 0, 0);
    const unsigned gxb = __builtin_amdgcn_raw_buffer_load_b32(xr, lane * 20 + 16, 0, 0);
    const rsrc_t gr = mkrsrc(gw, D * 2);
    const u32x4 gwa = __builtin_amdgcn_raw_buffer_load_b128(gr, lane * 20, 0, 0);
    const unsigned gwb = __builtin_amdgcn_raw_buffer_load_b32(gr, lane * 20 + 16, 0, 0);
    float z = 0.f;
#pragma unroll
    for (int k = 0; k < 4; ++k) { z = fmaf(bf_lo(gxa[k]), bf_lo(gwa[k]), z); z = fmaf(bf_hi(gxa[k]), bf_hi(gwa[k]), z); }
    z = fmaf(bf_lo(gxb), bf_lo(gwb), z); z = fmaf(bf_hi(gxb), bf_hi(gwb), z);
    z = sum_hi3(sum_lo3(z));
    gate = 1.f / (1.f + __expf(-z));
  }
  if (j == 2) {
    unsigned at = XCHK;
#pragma nounroll
    for (int st = 0; st < 2; ++st) {
      const unsigned pool = st ? NDYN2 : NDYN;
      lds_wait_ge<4>(&L.tcons, at);
      const unsigned old = L.tsn[st][lane & 7];
      const unsigned ta = WS_TG + (h * 8 + (lane & 7)) * TGS + 4 + st * 4;
      st4_sc1(wsr, lane == s ? ta : OOB, ((unsigned)rt() << 1) | (~old & 1u));
      unsigned tv = 0;
      bool seen = lane >= 8;
#pragma nounroll
      for (unsigned spin = 0; spin < GSPIN; ++spin) {
        __builtin_amdgcn_s_sleep(8);
        const unsigned cur = ld4_sc1(wsr, seen ? OOB : ta);
        if (!seen) { tv = cur; seen = ((cur ^ old) & 1u) != 0u; }
        if (__all(seen)) break;
      }
      const int rel = (int)((tv & ~1u) - (rfl(tv) & ~1u)) >> 1;
      const float lag = (float)rel - sum_lo3((float)rel) * 0.125f;
      float n = fmaxf((float)pool * 0.125f - lag * RATE, 0.f);
      n *= (float)pool / sum_lo3(n);
      unsigned cum = (unsigned)(n * 16.f);
      { unsigned t = udpp<SHR1>(cum); cum += (lane & 7) >= 1 ? t : 0u; t = udpp<SHR2>(cum); cum += (lane & 7) >= 2 ? t : 0u; t = udpp<SHR4>(cum); cum += (lane & 7) >= 4 ? t : 0u; }
      const unsigned hi = (lane & 7) == 7 ? pool : min((cum + 8u) >> 4, pool);
      const unsigned hprev = udpp<SHR1>(hi);
      const unsigned lo = (lane & 7) == 0 ? 0u : hprev;
      if (lane == s) lds_st(st ? &L.tprod2 : &L.tprod, 0x80000000u | (lo << 12) | (hi - lo));
      at = NSW + __builtin_amdgcn_readlane(hi - lo, s) - R2;
    }
  }
  if (j == 3) {
    const int part = lane >> 4, mr = lane & 15;
    const unsigned o = part < 3 ? WS_FO + (mr * 1536 + grp * 12 + part * 4) * 4 : OOB;
    lds_wait_ge<8>(&L.bdone, 1u);
#pragma nounroll
    while (rt() - tstart < FFN_T && !rfl(lds_ld(&L.sflag[0]))) __builtin_amdgcn_s_sleep(32);
    u32x4 a;
#pragma nounroll
    for (unsigned spin = 0; spin < GSPIN; ++spin) {
      CFENCE();
      a = ld_sc1(wsr, o);
      if (__all(valid4(a))) break;
      __builtin_amdgcn_s_sleep(16);
    }
    float hv[4];
#pragma unroll
    for (int k = 0; k < 4; ++k) {
      const float t = sum_lo3(__uint_as_float(a[k]));
      const float o8 = dpp<XOR8>(t);
      const float gv = (mr & 8) ? o8 : t, uv = (mr & 8) ? t : o8;
      hv[k] = gv / (1.f + __expf(-gv)) * uv;
    }
    if (mr == 0) *(f32x4*)&L.hs[part * 4] = f32x4{hv[0], hv[1], hv[2], hv[3]};
    CFENCE();
    if (lane == 0) lds_st(&L.hrdy, 1u);
  }
  if (j == 1) {
#pragma nounroll
    while (rt() - tstart < FFN_T && !rfl(lds_ld(&L.sflag[0]))) __builtin_amdgcn_s_sleep(32);
    const unsigned touch = __builtin_amdgcn_raw_buffer_load_b32(mkrsrc(out, D * 2), lane == 0 ? (b % 160) * 8 : OOB, 0, AUX_SC1);
    asm volatile("" :: "v"(touch));
  }
  if (j != 0) {
#pragma nounroll
    for (unsigned spin = 0; spin < LSPIN; ++spin) {
      const u32x4 f = *(volatile u32x4*)&L.sflag[0];
      if (rfl(f.x | f.y | f.z | f.w)) break;
      __builtin_amdgcn_s_sleep(2);
    }
    CFENCE();
    load_tiles();
  }
  f32x2 wf[6][4];
#pragma unroll
  for (int t = 0; t < 6; ++t)
#pragma unroll
    for (int k = 0; k < 4; ++k) { wf[t][k] = f32x2{bf_lo(wot[t][k]), bf_hi(wot[t][k])}; asm volatile("" : "+v"(wf[t][k])); }
  lds_wait_ge<8>(&L.hrdy, 1u);
  f32x2 y2[4];
  {
    const float h0 = L.hs[rg] * (1.f - gate), h1 = L.hs[8 + rg] * (1.f - gate);
#pragma unroll
    for (int k = 0; k < 4; ++k) y2[k] = f32x2{h0 * bf_lo(w2t[0][k]) + h1 * bf_lo(w2t[1][k]), h0 * bf_hi(w2t[0][k]) + h1 * bf_hi(w2t[1][k])};
  }
  STAMP(3);
  if (j == 0) {
#pragma nounroll
    for (unsigned spin = 0; spin < LSPIN; ++spin) {
      const u32x4 f = *(volatile u32x4*)&L.sflag[0];
      if (rfl(f.x & f.y & f.z & f.w)) break;
      if (!rfl(f.x | f.y | f.z | f.w)) __builtin_amdgcn_s_sleep(2);
    }
    CFENCE();
    STAMP(4);
    {
      const int hf = lane & 1;
      f32x4 pv[NSTR]; f32x2 pm[NSTR];
#pragma unroll
      for (int q2 = 0; q2 < NSTR; ++q2) { pv[q2] = *(const f32x4*)&L.mrg[q2][lane * 4]; pm[q2] = *(const f32x2*)&L.ml[q2][0]; }
      float MM = pm[0].x;
#pragma unroll
      for (int q2 = 1; q2 < NSTR; ++q2) MM = fmaxf(MM, pm[q2].x);
      float o0 = 0.f, o1 = 0.f, o2 = 0.f, o3 = 0.f;
#pragma unroll
      for (int q2 = 0; q2 < NSTR; ++q2) {
        const float ww = __builtin_amdgcn_exp2f(pm[q2].x - MM);
        o0 = fmaf(ww, pv[q2].x, o0); o1 = fmaf(ww, pv[q2].y, o1);
        o2 = fmaf(ww, pv[q2].z, o2); o3 = fmaf(ww, hf ? pm[q2].y : pv[q2].w, o3);
      }
      if (hf) o2 = MM;
      st_sc1(wsr, WS_P1 + b * 1024 + lane * 16, u32x4{san(o0), san(o1), san(o2), san(o3)});
    }
    STAMP(7);
    const unsigned o = WS_P1 + (h * 8 + c) * 1024 + (rq * 8 + rg) * 32;
    u32x4 A = {0u, 0u, 0u, 0u}, B = {0u, 0u, 0u, 0u};
    bool ok = false;
#pragma nounroll
    for (unsigned spin = 0; spin < GSPIN; ++spin) {
      CFENCE();
      const u32x4 nA = ld_sc1(wsr, ok ? OOB : o), nB = ld_sc1(wsr, ok ? OOB : o + 16);
      if (!ok) { A = nA; B = nB; ok = valid4(nA) && valid4(nB); }
      if (__all(ok)) break;
    }
    STAMP(5);
    const float mp = __uint_as_float(B.z);
    const float M = max_lo3(mp);
    const float w = __builtin_amdgcn_exp2f(mp - M);
    const float sc = gate * __builtin_amdgcn_rcpf(sum_lo3(w * __uint_as_float(B.w)));
    const float o0 = sum_lo3(w * __uint_as_float(A.x)) * sc, o1 = sum_lo3(w * __uint_as_float(A.y)) * sc, o2 = sum_lo3(w * __uint_as_float(A.z)) * sc;
    const float o3 = sum_lo3(w * __uint_as_float(A.w)) * sc, o4 = sum_lo3(w * __uint_as_float(B.x)) * sc, o5 = sum_lo3(w * __uint_as_float(B.y)) * sc;
    if (c == 0) { *(f32x4*)&L.os[rg * 8] = f32x4{o0, o1, o2, o3}; *(f32x2*)&L.os[rg * 8 + 4] = f32x2{o4, o5}; }
    CFENCE();
    if (lane == 0) lds_st(&L.oflag, 1u);
    CFENCE();
  } else {
    lds_wait_ge<4>(&L.sflag[0], 1u);
    lds_wait_ge<0>(&L.oflag, 1u);
  }
  {
    const f32x4 oa = *(const f32x4*)&L.os[rg * 8];
    const f32x2 ob = *(const f32x2*)&L.os[rg * 8 + 4];
#pragma unroll
    for (int t = 0; t < 6; ++t) {
      const float ov = t < 4 ? oa[t] : ob[t - 4];
#pragma unroll
      for (int k = 0; k < 4; ++k) y2[k] = __builtin_elementwise_fma(f32x2{ov, ov}, wf[t][k], y2[k]);
    }
    float r4[4];
#pragma unroll
    for (int k = 0; k < 4; ++k) { float t = tr32(y2[k >> 1][k & 1], y2[2 + (k >> 1)][k & 1]); t = tr16(t, t); r4[k] = t + dpp<XOR8>(t); }
    st_sc1(wsr, wr16 ? WS_P2 + (half * 80 + j * 16 + (col4 >> 2)) * 2048 + grp * 16 : OOB, u32x4{san(r4[0]), san(r4[1]), san(r4[2]), san(r4[3])});
  }
  STAMP(6);
  if (j == 1 && b < 160) {
    const unsigned o0 = WS_P2 + b * 2048 + lane * 16;
    u32x4 a0 = {0u, 0u, 0u, 0u}, a1 = {0u, 0u, 0u, 0u};
    bool k0 = false, k1 = false;
#pragma nounroll
    for (unsigned spin = 0; spin < GSPIN; ++spin) {
      CFENCE();
      const u32x4 n0 = ld_sc1(wsr, k0 ? OOB : o0), n1 = ld_sc1(wsr, k1 ? OOB : o0 + 1024);
      if (!k0) { a0 = n0; k0 = valid4(n0); }
      if (!k1) { a1 = n1; k1 = valid4(n1); }
      if (__all(k0 && k1)) break;
    }
    const float v0 = __uint_as_float(a0.x) + __uint_as_float(a1.x), v1 = __uint_as_float(a0.y) + __uint_as_float(a1.y);
    const float v2 = __uint_as_float(a0.z) + __uint_as_float(a1.z), v3 = __uint_as_float(a0.w) + __uint_as_float(a1.w);
    float t = tr16(tr32(v0, v2), tr32(v1, v3));
    t += dpp<XOR8>(t); t = sum_lo3(t);
    const __bf16 ov = (__bf16)t;
    __builtin_amdgcn_raw_buffer_store_b16(__builtin_bit_cast(unsigned short, ov), mkrsrc(out, D * 2), (lane & 15) == 0 ? (b * 4 + (lane >> 4)) * 2 : OOB, 0, 0);
    STAMP(7);
  }
  STAMP_FLUSH();
}

extern "C" void kernel_launch(void* const* d_in, const int* in_sizes, int n_in, void* d_out, int out_size, void* d_ws, size_t ws_size,
                              hipStream_t stream) {
  fused_kernel<<<256, NTHR, 0, stream>>>((const uint16_t*)d_in[0], (const uint16_t*)d_in[1], (const uint16_t*)d_in[2], (const uint16_t*)d_in[3],
                                         (const uint16_t*)d_in[4], (const uint16_t*)d_in[6], (const uint16_t*)d_in[7], (const uint16_t*)d_in[8],
                                         (const uint16_t*)d_in[9], (unsigned char*)d_ws, (uint16_t*)d_out);
}
```

```cpp
#include <hip/hip_runtime.h>
#include <hip/hip_bf16.h>
#include <stdint.h>
#include <stddef.h>

typedef unsigned u32x4 __attribute__((ext_vector_type(4)));
typedef unsigned u32x2 __attribute__((ext_vector_type(2)));
typedef float f32x4 __attribute__((ext_vector_type(4)));
typedef float f32x2 __attribute__((ext_vector_type(2)));
typedef __bf16 bf16x2 __attribute__((ext_vector_type(2)));
typedef __amdgpu_buffer_rsrc_t rsrc_t;

constexpr int RING = 4;
constexpr int AHEAD = 1;
constexpr int PREW = 3;
constexpr int PMAX = 6;
constexpr unsigned NDYN = 128, NDYN2 = 64;
constexpr unsigned XCHK = 28;
constexpr unsigned R2 = 16;
constexpr float RATE = 0.05f;
constexpr unsigned FFN_T = 2000;

constexpr int D = 640, H = 32, SK = 8192, DH = 192, F = 1536;
constexpr int NHELP = 5, NSTR = 4, NWAVES = NHELP + NSTR, NTHR = NWAVES * 64;
constexpr int AUX_NT = 2, AUX_SC1 = 16;
constexpr unsigned OOB = 0x7ffffff0u;
constexpr unsigned WS_QO = 0;
constexpr unsigned WS_FO = WS_QO + 256 * 192 * 4;
constexpr unsigned WS_P1 = WS_FO + 16 * 1536 * 4;
constexpr unsigned WS_P2 = WS_P1 + 256 * 1024;
constexpr unsigned WS_TG = WS_P2 + 160 * 2048;
constexpr unsigned TGS = 128;
constexpr unsigned WS_END = WS_TG + 256 * TGS;
constexpr unsigned WS_STAMP = 2u << 20;
constexpr unsigned SENT = 0xFFFFFFFFu;
constexpr unsigned NWH = 8 * NSTR, STAT = 1024 - NDYN - NDYN2, UPW = STAT / 8;
constexpr unsigned R0 = RING + PMAX + AHEAD;
constexpr unsigned NSW = UPW - R0 * NSTR;
static_assert(STAT % NWH == 0 && UPW >= R0 * NSTR && AHEAD < RING && XCHK <= NSW && R2 <= NSW - XCHK, "work split");

__device__ __forceinline__ float bf_lo(unsigned u) { return __builtin_bit_cast(float, u << 16); }
__device__ __forceinline__ float bf_hi(unsigned u) { return __builtin_bit_cast(float, u & 0xffff0000u); }
__device__ __forceinline__ float dot2(unsigned k, unsigned q, float acc) {
  return __builtin_amdgcn_fdot2_f32_bf16(__builtin_bit_cast(bf16x2, k), __builtin_bit_cast(bf16x2, q), acc, false);
}
template <int CTRL> __device__ __forceinline__ float dpp(float x) {
  return __builtin_bit_cast(float, __builtin_amdgcn_mov_dpp(__builtin_bit_cast(int, x), CTRL, 0xf, 0xf, true));
}
template <int CTRL> __device__ __forceinline__ unsigned udpp(unsigned x) { return (unsigned)__builtin_amdgcn_mov_dpp((int)x, CTRL, 0xf, 0xf, true); }
constexpr int XOR1 = 0xB1, XOR2 = 0x4E, HMIR = 0x141, XOR8 = 0x128, SHR1 = 0x111, SHR2 = 0x112, SHR4 = 0x114;
__device__ __forceinline__ float tr16(float a, float b) {
  auto s = __builtin_amdgcn_permlane16_swap(__float_as_uint(a), __float_as_uint(b), false, false);
  return __uint_as_float(s[0]) + __uint_as_float(s[1]);
}
__device__ __forceinline__ float tr32(float a, float b) {
  auto s = __builtin_amdgcn_permlane32_swap(__float_as_uint(a), __float_as_uint(b), false, false);
  return __uint_as_float(s[0]) + __uint_as_float(s[1]);
}
__device__ __forceinline__ float max16(float x) {
  auto s = __builtin_amdgcn_permlane16_swap(__float_as_uint(x), __float_as_uint(x), false, false);
  return fmaxf(__uint_as_float(s[0]), __uint_as_float(s[1]));
}
__device__ __forceinline__ float max32(float x) {
  auto s = __builtin_amdgcn_permlane32_swap(__float_as_uint(x), __float_as_uint(x), false, false);
  return fmaxf(__uint_as_float(s[0]), __uint_as_float(s[1]));
}
__device__ __forceinline__ float sum_hi3(float x) { x += dpp<XOR8>(x); x = tr16(x, x); return tr32(x, x); }
__device__ __forceinline__ float sum_lo3(float x) { x += dpp<XOR1>(x); x += dpp<XOR2>(x); x += dpp<HMIR>(x); return x; }
__device__ __forceinline__ float max_lo3(float x) { x = fmaxf(x, dpp<XOR1>(x)); x = fmaxf(x, dpp<XOR2>(x)); return fmaxf(x, dpp<HMIR>(x)); }
__device__ __forceinline__ unsigned san(float v) { unsigned u = __float_as_uint(v); return u < 0xFFFFFFFEu ? u : 0xFFFFFFFEu; }
__device__ __forceinline__ rsrc_t mkrsrc(const void* p, unsigned bytes) { return __builtin_amdgcn_make_buffer_rsrc((void*)p, 0, bytes, 0x00020000); }
__device__ __forceinline__ u32x4 ld_nt(rsrc_t r, unsigned voff) { return __builtin_amdgcn_raw_buffer_load_b128(r, voff, 0, AUX_NT); }
__device__ __forceinline__ u32x4 ld_sc1(rsrc_t r, unsigned voff) { return __builtin_amdgcn_raw_buffer_load_b128(r, voff, 0, AUX_SC1); }
__device__ __forceinline__ unsigned ld4_sc1(rsrc_t r, unsigned voff) { return __builtin_amdgcn_raw_buffer_load_b32(r, voff, 0, AUX_SC1); }
__device__ __forceinline__ void st_sc1(rsrc_t r, unsigned voff, u32x4 v) { __builtin_amdgcn_raw_buffer_store_b128(v, r, voff, 0, AUX_SC1); }
__device__ __forceinline__ void st4_sc1(rsrc_t r, unsigned voff, unsigned v) { __builtin_amdgcn_raw_buffer_store_b32(v, r, voff, 0, AUX_SC1); }
__device__ __forceinline__ bool valid4(u32x4 v) { return v.x != SENT && v.y != SENT && v.z != SENT && v.w != SENT; }
__device__ __forceinline__ unsigned long long rt() { return __builtin_amdgcn_s_memrealtime(); }
__device__ __forceinline__ unsigned lds_ld(const unsigned* p) { return __hip_atomic_load(p, __ATOMIC_RELAXED, __HIP_MEMORY_SCOPE_WORKGROUP); }
__device__ __forceinline__ void lds_st(unsigned* p, unsigned v) { __hip_atomic_store(p, v, __ATOMIC_RELAXED, __HIP_MEMORY_SCOPE_WORKGROUP); }
__device__ __forceinline__ unsigned lds_add(unsigned* p) { return __hip_atomic_fetch_add(p, 1u, __ATOMIC_RELAXED, __HIP_MEMORY_SCOPE_WORKGROUP); }
__device__ __forceinline__ unsigned rfl(unsigned v) { return __builtin_amdgcn_readfirstlane(v); }
constexpr unsigned GSPIN = 1u << 15;
constexpr unsigned LSPIN = 1u << 19;
template <int SLEEP> __device__ __forceinline__ void lds_wait_ge(const unsigned* p, unsigned v) {
#pragma nounroll
  for (unsigned spin = 0; spin < LSPIN; ++spin) {
    if (rfl(lds_ld(p)) >= v) break;
    __builtin_amdgcn_s_sleep(SLEEP);
  }
  asm volatile("" ::: "memory");
}
#define WAIT_VM0() asm volatile("s_waitcnt vmcnt(0)" ::: "memory")
#define WAIT_LKM0() asm volatile("s_waitcnt lgkmcnt(0)" ::: "memory")
#define CFENCE() asm volatile("" ::: "memory")

#define STAMP(i) do {} while (0)
#define STAMP_FLUSH() do {} while (0)

template <int N> struct IC { static constexpr int value = N; };
template <int B, int E, class Fn> __device__ __forceinline__ void static_for(Fn&& f) { if constexpr (B < E) { f(IC<B>{}); static_for<B + 1, E>(f); } }

struct Lds {
  float mrg[NSTR][256];
  float ml[NSTR][2];
  unsigned qs[96];
  float os[64];
  float hs[16];
  unsigned qcnt;
  unsigned pre;
  unsigned qrdy;
  unsigned bdone;
  unsigned hrdy;
  unsigned oflag;
  unsigned tcons;
  unsigned tprod;
  unsigned sflag[4];
  unsigned tsn[2][8];
  unsigned tprod2, pad2[3];
  u32x4 park[NSTR][PMAX][6][64];
};
static_assert(offsetof(Lds, sflag) % 16 == 0 && offsetof(Lds, os) % 16 == 0 && offsetof(Lds, qs) % 16 == 0 && offsetof(Lds, hs) % 16 == 0 && offsetof(Lds, park) % 16 == 0, "LDS alignment");
static_assert(sizeof(Lds) <= 160 * 1024, "LDS size");

struct GemvLoads { u32x4 w[10]; unsigned xs; };
__device__ __forceinline__ void gemv_issue(GemvLoads& g, rsrc_t xr, rsrc_t wr, unsigned ldB, unsigned row0, unsigned colB, int lane) {
  const unsigned off = (row0 + (lane >> 3)) * ldB + colB + (lane & 7) * 16;
#pragma unroll
  for (int i = 0; i < 10; ++i) g.w[i] = ld_nt(wr, off + i * 8 * ldB);
  g.xs = __builtin_amdgcn_raw_buffer_load_b32(xr, lane < 40 ? row0 * 2 + lane * 4 : OOB, 0, 0);
}
__device__ __forceinline__ void gemv_compute(const GemvLoads& g, float (&r)[4], int lane) {
  float acc[8];
#pragma unroll
  for (int k = 0; k < 8; ++k) acc[k] = 0.f;
  const int rg = lane >> 3;
#pragma unroll
  for (int i = 0; i < 10; ++i) {
    const unsigned xw = (unsigned)__builtin_amdgcn_ds_bpermute((i * 4 + (rg >> 1)) * 4, (int)g.xs);
    const float xv = (rg & 1) ? bf_hi(xw) : bf_lo(xw);
#pragma unroll
    for (int k = 0; k < 4; ++k) { acc[2 * k] = fmaf(xv, bf_lo(g.w[i][k]), acc[2 * k]); acc[2 * k + 1] = fmaf(xv, bf_hi(g.w[i][k]), acc[2 * k + 1]); }
  }
#pragma unroll
  for (int k = 0; k < 4; ++k) { float t = tr32(acc[k], acc[k + 4]); t = tr16(t, t); r[k] = t + dpp<XOR8>(t); }
}

__global__ void __launch_bounds__(NTHR) fused_kernel(const uint16_t* __restrict__ x, const uint16_t* __restrict__ kc, const uint16_t* __restrict__ vc,
                                                     const uint16_t* __restrict__ wq, const uint16_t* __restrict__ wo, const uint16_t* __restrict__ w1,
                                                     const uint16_t* __restrict__ w3, const uint16_t* __restrict__ w2, const uint16_t* __restrict__ gw,
                                                     unsigned char* __restrict__ ws, uint16_t* __restrict__ out) {
  __shared__ __attribute__((aligned(16))) Lds L;
  const int b = blockIdx.x, h = b >> 3, s = b & 7;
  const int tid = threadIdx.x, lane = tid & 63, wave = __builtin_amdgcn_readfirstlane(tid >> 6);
  const int rg = lane >> 3, c = lane & 7;
  STAMP(0);
  asm volatile("" :: "s"(x), "s"(kc), "s"(vc), "s"(wq), "s"(wo), "s"(w1), "s"(w3), "s"(w2), "s"(gw), "s"(ws), "s"(out));
  const rsrc_t wsr = mkrsrc(ws, WS_END);

  if (wave >= NHELP) {
    const int sw = wave - NHELP;
    const unsigned long long kb = (unsigned long long)(kc + (size_t)h * SK * DH), vb = (unsigned long long)(vc + (size_t)h * SK * DH);
    const u32x4 kr = {(unsigned)kb, (unsigned)(kb >> 32) & 0xffffu, (unsigned)(SK * DH * 2), 0x00020000u};
    const u32x4 vr = {(unsigned)vb, (unsigned)(vb >> 32) & 0xffffu, (unsigned)(SK * DH * 2), 0x00020000u};
    const unsigned loff = (unsigned)(rg * DH + c * 8) * 2;
    constexpr unsigned STEPB = 8 * DH * 2;
    u32x4 K[RING][3], V[RING][3];
#define RING_LOADS "s_nop 4\n\t" \
                   "buffer_load_dwordx4 %0, %6, %7, %9 offen nt\n\t" \
                   "buffer_load_dwordx4 %1, %6, %7, %9 offen offset:128 nt\n\t" \
                   "buffer_load_dwordx4 %2, %6, %7, %9 offen offset:256 nt\n\t" \
                   "buffer_load_dwordx4 %3, %6, %8, %9 offen nt\n\t" \
                   "buffer_load_dwordx4 %4, %6, %8, %9 offen offset:128 nt\n\t" \
                   "buffer_load_dwordx4 %5, %6, %8, %9 offen offset:256 nt"
    auto issue0 = [&](int slot, unsigned unit) {
      const unsigned so = unit * STEPB;
      asm volatile(RING_LOADS
                   : "=&v"(K[slot][0]), "=&v"(K[slot][1]), "=&v"(K[slot][2]), "=&v"(V[slot][0]), "=&v"(V[slot][1]), "=&v"(V[slot][2])
                   : "v"(loff), "s"(kr), "s"(vr), "s"(so));
    };
    auto issue = [&](int slot, unsigned unit) {
      const unsigned so = unit * STEPB;
      asm volatile(RING_LOADS
                   : "+v"(K[slot][0]), "+v"(K[slot][1]), "+v"(K[slot][2]), "+v"(V[slot][0]), "+v"(V[slot][1]), "+v"(V[slot][2])
                   : "v"(loff), "s"(kr), "s"(vr), "s"(so));
    };
#pragma unroll
    for (int i = 0; i < RING; ++i) {
      if (i >= 1) asm volatile("s_waitcnt vmcnt(%0)" :: "i"(PREW) : "memory");
      issue0(i, i * NWH + s * NSTR + sw);
      __builtin_amdgcn_sched_barrier(0);
      if (i == 0) { __builtin_amdgcn_s_barrier(); __builtin_amdgcn_sched_barrier(0); }
    }
    unsigned pi = RING;
    auto private_unit = [&]() -> unsigned { return (pi++) * NWH + s * NSTR + sw; };
    int np = 0;
    {
      const unsigned pbase = rfl((unsigned)(size_t)&L.park[sw][0][0][0]);
#pragma nounroll
      while (np < PMAX) {
        asm volatile("s_waitcnt vmcnt(%0)" :: "i"(PREW) : "memory");
        if (rfl(lds_ld(&L.qcnt)) >= 3u) break;
        const unsigned so = private_unit() * STEPB, mb = pbase + np * 6144;
        unsigned keep;
        asm volatile("s_mov_b32 %0, m0\n\t"
                     "s_mov_b32 m0, %1\n\ts_nop 4\n\t"
                     "buffer_load_dwordx4 %2, %3, %5 offen nt lds\n\t"
                     "s_add_u32 m0, %1, 896\n\ts_nop 0\n\t"
                     "buffer_load_dwordx4 %2, %3, %5 offen offset:128 nt lds\n\t"
                     "s_add_u32 m0, %1, 1792\n\ts_nop 0\n\t"
                     "buffer_load_dwordx4 %2, %3, %5 offen offset:256 nt lds\n\t"
                     "s_add_u32 m0, %1, 3072\n\ts_nop 0\n\t"
                     "buffer_load_dwordx4 %2, %4, %5 offen nt lds\n\t"
                     "s_add_u32 m0, %1, 3968\n\ts_nop 0\n\t"
                     "buffer_load_dwordx4 %2, %4, %5 offen offset:128 nt lds\n\t"
                     "s_add_u32 m0, %1, 4864\n\ts_nop 0\n\t"
                     "buffer_load_dwordx4 %2, %4, %5 offen offset:256 nt lds\n\t"
                     "s_mov_b32 m0, %0"
                     : "=&s"(keep) : "s"(mb), "v"(loff), "s"(kr), "s"(vr), "s"(so) : "scc", "memory");
        ++np;
      }
    }
    STAMP(1);
    constexpr float C2 = 0.07216878364870322f * 1.4426950408889634f;
    float m = -INFINITY, l = 0.f, p_, alpha_;
    f32x2 acc2[12];
#pragma unroll
    for (int i = 0; i < 12; ++i) acc2[i] = f32x2{0.f, 0.f};
    u32x4 qp[3];
    auto scores = [&](const u32x4 (&Kx)[3]) {
      float d3[3];
#pragma unroll
      for (int jj = 0; jj < 3; ++jj) {
        d3[jj] = 0.f;
#pragma unroll
        for (int k = 0; k < 4; ++k) d3[jj] = dot2(Kx[jj][k], qp[jj][k], d3[jj]);
      }
      const float sc = sum_lo3(d3[0] + d3[1] + d3[2]) * C2;
      float pm = fmaxf(sc, dpp<XOR8>(sc)); pm = max32(max16(pm));
      const float mn = fmaxf(m, pm);
      alpha_ = __builtin_amdgcn_exp2f(m - mn); p_ = __builtin_amdgcn_exp2f(sc - mn);
      l = fmaf(l, alpha_, p_); m = mn;
    };
    auto accum = [&](const u32x4 (&Vx)[3]) {
      if (rfl(__float_as_uint(alpha_)) != 0x3f800000u) {
#pragma unroll
        for (int i = 0; i < 12; ++i) acc2[i] *= f32x2{alpha_, alpha_};
      }
#pragma unroll
      for (int jj = 0; jj < 3; ++jj)
#pragma unroll
        for (int k = 0; k < 4; ++k) acc2[jj * 4 + k] = __builtin_elementwise_fma(f32x2{p_, p_}, f32x2{bf_lo(Vx[jj][k]), bf_hi(Vx[jj][k])}, acc2[jj * 4 + k]);
#pragma unroll
      for (int jj = 0; jj < 3; ++jj) asm volatile("" : "+v"(acc2[jj * 4]), "+v"(acc2[jj * 4 + 1]), "+v"(acc2[jj * 4 + 2]), "+v"(acc2[jj * 4 + 3]));
    };
    auto consume = [&](int slot, auto younger) {
      constexpr int YN = decltype(younger)::value * 6;
      asm volatile("s_waitcnt vmcnt(%3)" : "+v"(K[slot][0]), "+v"(K[slot][1]), "+v"(K[slot][2]) : "i"(YN + 3));
      scores(K[slot]);
      asm volatile("s_waitcnt vmcnt(%3)" : "+v"(V[slot][0]), "+v"(V[slot][1]), "+v"(V[slot][2]) : "i"(YN));
      accum(V[slot]);
    };
    auto consume_parked = [&](int ip) {
      u32x4 T[3];
#pragma unroll
      for (int jj = 0; jj < 3; ++jj) T[jj] = L.park[sw][ip][jj][lane];
      scores(T);
      __builtin_amdgcn_sched_barrier(0);
#pragma unroll
      for (int jj = 0; jj < 3; ++jj) T[jj] = L.park[sw][ip][3 + jj][lane];
      accum(T);
    };
    lds_wait_ge<1>(&L.qcnt, 3u);
#pragma unroll
    for (int jj = 0; jj < 3; ++jj) qp[jj] = *(const u32x4*)&L.qs[jj * 32 + c * 4];
    STAMP(2);
    static_for<0, RING>([&](auto ic) {
      constexpr int i = decltype(ic)::value;
      consume(i, IC<RING - 1 - i + (i < AHEAD ? i : AHEAD)>{});
      if constexpr (i < AHEAD) issue(i, private_unit());
      __builtin_amdgcn_sched_barrier(0);
    });
    int rem = AHEAD;
    bool more = true;
#pragma nounroll
    do {
#pragma unroll
      for (int k = 0; k < RING; ++k) {
        if (rem > 0) {
          unsigned un = 1024u;
          if (pi < R0) un = private_unit();
          else if (more) {
            unsigned idx = 0;
            if (lane == 0) idx = lds_add(&L.tcons);
            idx = rfl(idx);
            if (idx < NSW) un = (R0 + idx / NSTR) * NWH + s * NSTR + idx % NSTR;
            else {
              idx -= NSW;
#pragma nounroll
              for (unsigned spin = 0; spin < LSPIN; ++spin) {
                const unsigned pr = rfl(lds_ld(&L.tprod));
                if (pr >> 31) {
                  if (idx < (pr & 0xfffu)) { un = STAT + ((pr >> 12) & 0xfffu) + idx; break; }
                  const unsigned p2 = rfl(lds_ld(&L.tprod2)), i2 = idx - (pr & 0xfffu);
                  if (p2 >> 31) { if (i2 < (p2 & 0xfffu)) un = STAT + NDYN + ((p2 >> 12) & 0xfffu) + i2; break; }
                }
                __builtin_amdgcn_s_sleep(1);
              }
            }
          }
          more = un < 1024u;
          issue((k + AHEAD) % RING, un);
          rem += more ? 0 : -1;
          consume(k, IC<AHEAD>{});
          __builtin_amdgcn_sched_barrier(0);
        }
      }
      if (np > 0) { CFENCE(); --np; consume_parked(np); __builtin_amdgcn_sched_barrier(0); }
    } while (rem > 0 || np > 0);
#pragma unroll
    for (int r2 = 0; r2 < RING; ++r2)
      asm volatile("s_waitcnt vmcnt(0)" :: "v"(K[r2][0]), "v"(K[r2][1]), "v"(K[r2][2]), "v"(V[r2][0]), "v"(V[r2][1]), "v"(V[r2][2]));
    __builtin_amdgcn_sched_barrier(0);
    STAMP(3);
    float r6[6];
    {
      float r12[12];
#pragma unroll
      for (int i = 0; i < 12; ++i) r12[i] = tr32(acc2[i >> 1][i & 1], acc2[6 + (i >> 1)][i & 1]);
#pragma unroll
      for (int i = 0; i < 6; ++i) { const float t = tr16(r12[i], r12[i + 6]); r6[i] = t + dpp<XOR8>(t); }
    }
    const float lw = sum_hi3(l);
    if ((lane & 8) == 0) {
      const int slot = c * 4 + (lane >> 4);
      *(f32x4*)&L.mrg[sw][slot * 8] = f32x4{r6[0], r6[1], r6[2], r6[3]};
      *(f32x2*)&L.mrg[sw][slot * 8 + 4] = f32x2{r6[4], r6[5]};
    } else if (lane == 8) *(f32x2*)&L.ml[sw][0] = f32x2{m, lw};
    CFENCE();
    if (lane == 0) lds_st(&L.sflag[sw], 1u);
    STAMP(4);
    STAMP_FLUSH();
    return;
  }

  const unsigned long long tstart = rt();
  const rsrc_t xr = mkrsrc(x, D * 2);
  const int j = wave;
  const int rq = s >> 1, half = s & 1;
  const bool wr16 = (lane & 0x18) == 0;
  const unsigned col4 = c * 8 + (lane >> 5) * 4;
  const unsigned grp = h * 4 + rq;
  u32x4 wot[6], w2t[2];
#pragma unroll
  for (int t = 0; t < 6; ++t) wot[t] = u32x4{0u, 0u, 0u, 0u};
  w2t[0] = w2t[1] = u32x4{0u, 0u, 0u, 0u};
  auto load_tiles = [&]() {
    const unsigned colB = (half * 320 + j * 64 + c * 8) * 2;
    const rsrc_t wor = mkrsrc(wo, H * DH * D * 2);
    const int cs = rq * 2 + (rg >> 2), i0 = (rg & 3) * 6;
#pragma unroll
    for (int t = 0; t < 6; ++t) { const int i = i0 + t; const unsigned d = (i >> 3) * 64 + cs * 8 + (i & 7); wot[t] = ld_nt(wor, (h * DH + d) * (D * 2) + colB); }
    const rsrc_t w2r = mkrsrc(w2, F * D * 2);
    w2t[0] = ld_nt(w2r, (grp * 12 + rg) * (D * 2) + colB);
    w2t[1] = ld_nt(w2r, rg < 4 ? (grp * 12 + 8 + rg) * (D * 2) + colB : OOB);
  };
  {
    GemvLoads g;
    float pa[4];
    if (j < 3) {
      const int p = lane >> 4, c4 = lane & 15;
      const unsigned o0 = WS_QO + ((h * 8 + p) * 192 + j * 64 + c4 * 4) * 4, o1 = o0 + 4 * 192 * 4;
      const unsigned om = wr16 ? WS_QO + (b * 192 + j * 64 + col4) * 4 : OOB;
      gemv_issue(g, xr, mkrsrc(wq, D * H * DH * 2), H * DH * 2, s * 80, (h * DH + j * 64) * 2, lane);
      __builtin_amdgcn_sched_barrier(0);
      if (tid < 12) (&L.qcnt)[tid] = 0u;
      if (tid == 12) L.tprod2 = 0u;
      WAIT_LKM0();
      __builtin_amdgcn_s_barrier();
      __builtin_amdgcn_sched_barrier(0);
      gemv_compute(g, pa, lane);
      STAMP(1);
      st_sc1(wsr, om, u32x4{__float_as_uint(pa[0]), __float_as_uint(pa[1]), __float_as_uint(pa[2]), __float_as_uint(pa[3])});
      WAIT_VM0();
      if (lane == 0) lds_add(&L.pre);
      lds_wait_ge<1>(&L.qrdy, 1u);
      const u32x4 a0 = ld_sc1(wsr, o0), a1 = ld_sc1(wsr, o1);
      float q[4];
#pragma unroll
      for (int k = 0; k < 4; ++k) { float t = __uint_as_float(a0[k]) + __uint_as_float(a1[k]); t = tr16(t, t); q[k] = tr32(t, t); }
      if (p == 0) {
        bf16x2 lo = {(__bf16)q[0], (__bf16)q[1]}, hi = {(__bf16)q[2], (__bf16)q[3]};
        *(u32x2*)&L.qs[j * 32 + c4 * 2] = u32x2{__builtin_bit_cast(unsigned, lo), __builtin_bit_cast(unsigned, hi)};
      }
      CFENCE();
      if (lane == 0) lds_add(&L.qcnt);
      STAMP(2);
      if (j == 0) load_tiles();
    } else {
      const int unit = j == 3 ? b : ((b & 1) ? -1 : 256 + (b >> 1));
      const unsigned u = unit < 0 ? 0 : unit, mat = u / 192, lb = (u % 192) >> 3, rp = u & 7;
      const unsigned tw = lane < 8 ? WS_TG + (h * 8 + lane) * TGS : (lane >= 32 && s == 0) ? WS_TG + (lane - 32) * 8 * TGS : OOB;
      unsigned snap = 0;
      if (j == 4) {
        const u32x4 snap2 = ld_sc1(wsr, tw);
        snap = snap2.x;
        const u32x4 sv = {SENT, SENT, SENT, SENT};
        st_sc1(wsr, WS_P1 + b * 1024 + lane * 16, sv);
        st_sc1(wsr, b < 160 ? WS_P2 + b * 2048 + lane * 16 : OOB, sv);
        st_sc1(wsr, b < 160 ? WS_P2 + b * 2048 + 1024 + lane * 16 : OOB, sv);
        const unsigned u2 = lane < 16 ? b : 256 + (b >> 1);
        const bool on = lane < 16 || (lane < 32 && !(b & 1));
        st_sc1(wsr, on ? WS_FO + (((u2 / 192) * 8 + (u2 & 7)) * 1536 + ((u2 % 192) >> 3) * 64 + (lane & 15) * 4) * 4 : OOB, sv);
        __builtin_amdgcn_sched_barrier(0);
        __builtin_amdgcn_s_barrier();
        WAIT_VM0();
        if (lane < 8) { L.tsn[0][lane] = snap2.y; L.tsn[1][lane] = snap2.z; }
        CFENCE();
      } else {
        __builtin_amdgcn_s_barrier();
      }
      if (j == 3) gemv_issue(g, xr, mkrsrc(mat ? w3 : w1, unit < 0 ? 0 : D * F * 2), F * 2, rp * 80, lb * 128, lane);
      if (j == 4) {
        lds_wait_ge<0>(&L.pre, 3u);
        st4_sc1(wsr, lane == s ? tw : OOB, snap ^ 8u);
        bool sq = lane >= 8;
#pragma nounroll
        for (unsigned spin = 0; spin < GSPIN; ++spin) {
          __builtin_amdgcn_s_sleep(4);
          const unsigned cur = ld4_sc1(wsr, sq ? OOB : tw);
          sq = sq || ((cur ^ snap) & 11u) != 0u;
          if (__all(sq)) break;
        }
        if (lane == 0) lds_st(&L.qrdy, 1u);
        CFENCE();
        st4_sc1(wsr, lane == s ? tw : OOB, snap ^ 9u);
        gemv_issue(g, xr, mkrsrc(mat ? w3 : w1, unit < 0 ? 0 : D * F * 2), F * 2, rp * 80, lb * 128, lane);
        STAMP(1);
        const unsigned want = s == 0 ? 3u : 4u;
        bool seen = s == 0 ? lane < 32 : lane != 0;
#pragma nounroll
        for (unsigned spin = 0; spin < GSPIN; ++spin) {
          __builtin_amdgcn_s_sleep(16);
          const unsigned cur = ld4_sc1(wsr, seen ? OOB : tw);
          seen = seen || ((cur ^ snap) & want) != 0u;
          if (__all(seen)) break;
        }
        st4_sc1(wsr, (s == 0 && lane == 0) ? tw : OOB, snap ^ 15u);
        if (lane == 0) lds_st(&L.bdone, 1u);
        STAMP(2);
      }
      gemv_compute(g, pa, lane);
      lds_wait_ge<4>(&L.qrdy, 1u);
      st_sc1(wsr, (wr16 && unit >= 0) ? WS_FO + ((mat * 8 + rp) * 1536 + lb * 64 + col4) * 4 : OOB, u32x4{san(pa[0]), san(pa[1]), san(pa[2]), san(pa[3])});
    }
  }
  float gate;
  {
    const u32x4 gxa = __builtin_amdgcn_raw_buffer_load_b128(xr, lane * 20, 0, 0);
    const unsigned gxb = __builtin_amdgcn_raw_buffer_load_b32(xr, lane * 20 + 16, 0, 0);
    const rsrc_t gr = mkrsrc(gw, D * 2);
    const u32x4 gwa = __builtin_amdgcn_raw_buffer_load_b128(gr, lane * 20, 0, 0);
    const unsigned gwb = __builtin_amdgcn_raw_buffer_load_b32(gr, lane * 20 + 16, 0, 0);
    float z = 0.f;
#pragma unroll
    for (int k = 0; k < 4; ++k) { z = fmaf(bf_lo(gxa[k]), bf_lo(gwa[k]), z); z = fmaf(bf_hi(gxa[k]), bf_hi(gwa[k]), z); }
    z = fmaf(bf_lo(gxb), bf_lo(gwb), z); z = fmaf(bf_hi(gxb), bf_hi(gwb), z);
    z = sum_hi3(sum_lo3(z));
    gate = 1.f / (1.f + __expf(-z));
  }
  if (j == 2) {
    unsigned at = XCHK;
#pragma nounroll
    for (int st = 0; st < 2; ++st) {
      const unsigned pool = st ? NDYN2 : NDYN;
      lds_wait_ge<4>(&L.tcons, at);
      const unsigned old = L.tsn[st][lane & 7];
      const unsigned ta = WS_TG + (h * 8 + (lane & 7)) * TGS + 4 + st * 4;
      st4_sc1(wsr, lane == s ? ta : OOB, ((unsigned)rt() << 1) | (~old & 1u));
      unsigned tv = 0;
      bool seen = lane >= 8;
#pragma nounroll
      for (unsigned spin = 0; spin < GSPIN; ++spin) {
        __builtin_amdgcn_s_sleep(8);
        const unsigned cur = ld4_sc1(wsr, seen ? OOB : ta);
        if (!seen) { tv = cur; seen = ((cur ^ old) & 1u) != 0u; }
        if (__all(seen)) break;
      }
      const int rel = (int)((tv & ~1u) - (rfl(tv) & ~1u)) >> 1;
      const float lag = (float)rel - sum_lo3((float)rel) * 0.125f;
      float n = fmaxf((float)pool * 0.125f - lag * RATE, 0.f);
      n *= (float)pool / sum_lo3(n);
      unsigned cum = (unsigned)(n * 16.f);
      { unsigned t = udpp<SHR1>(cum); cum += (lane & 7) >= 1 ? t : 0u; t = udpp<SHR2>(cum); cum += (lane & 7) >= 2 ? t : 0u; t = udpp<SHR4>(cum); cum += (lane & 7) >= 4 ? t : 0u; }
      const unsigned hi = (lane & 7) == 7 ? pool : min((cum + 8u) >> 4, pool);
      const unsigned hprev = udpp<SHR1>(hi);
      const unsigned lo = (lane & 7) == 0 ? 0u : hprev;
      if (lane == s) lds_st(st ? &L.tprod2 : &L.tprod, 0x80000000u | (lo << 12) | (hi - lo));
      at = NSW + __builtin_amdgcn_readlane(hi - lo, s) - R2;
    }
  }
  if (j == 3) {
    const int part = lane >> 4, mr = lane & 15;
    const unsigned o = part < 3 ? WS_FO + (mr * 1536 + grp * 12 + part * 4) * 4 : OOB;
    lds_wait_ge<8>(&L.bdone, 1u);
#pragma nounroll
    while (rt() - tstart < FFN_T && !rfl(lds_ld(&L.sflag[0]))) __builtin_amdgcn_s_sleep(32);
    u32x4 a;
#pragma nounroll
    for (unsigned spin = 0; spin < GSPIN; ++spin) {
      CFENCE();
      a = ld_sc1(wsr, o);
      if (__all(valid4(a))) break;
      __builtin_amdgcn_s_sleep(16);
    }
    float hv[4];
#pragma unroll
    for (int k = 0; k < 4; ++k) {
      const float t = sum_lo3(__uint_as_float(a[k]));
      const float o8 = dpp<XOR8>(t);
      const float gv = (mr & 8) ? o8 : t, uv = (mr & 8) ? t : o8;
      hv[k] = gv / (1.f + __expf(-gv)) * uv;
    }
    if (mr == 0) *(f32x4*)&L.hs[part * 4] = f32x4{hv[0], hv[1], hv[2], hv[3]};
    CFENCE();
    if (lane == 0) lds_st(&L.hrdy, 1u);
  }
  if (j == 1) {
#pragma nounroll
    while (rt() - tstart < FFN_T && !rfl(lds_ld(&L.sflag[0]))) __builtin_amdgcn_s_sleep(32);
    const unsigned touch = __builtin_amdgcn_raw_buffer_load_b32(mkrsrc(out, D * 2), lane == 0 ? (b % 160) * 8 : OOB, 0, AUX_SC1);
    asm volatile("" :: "v"(touch));
  }
  if (j != 0) {
#pragma nounroll
    for (unsigned spin = 0; spin < LSPIN; ++spin) {
      const u32x4 f = *(volatile u32x4*)&L.sflag[0];
      if (rfl(f.x | f.y | f.z | f.w)) break;
      __builtin_amdgcn_s_sleep(2);
    }
    CFENCE();
    load_tiles();
  }
  f32x2 wf[6][4];
#pragma unroll
  for (int t = 0; t < 6; ++t)
#pragma unroll
    for (int k = 0; k < 4; ++k) { wf[t][k] = f32x2{bf_lo(wot[t][k]), bf_hi(wot[t][k])}; asm volatile("" : "+v"(wf[t][k])); }
  lds_wait_ge<8>(&L.hrdy, 1u);
  f32x2 y2[4];
  {
    const float h0 = L.hs[rg] * (1.f - gate), h1 = L.hs[8 + rg] * (1.f - gate);
#pragma unroll
    for (int k = 0; k < 4; ++k) y2[k] = f32x2{h0 * bf_lo(w2t[0][k]) + h1 * bf_lo(w2t[1][k]), h0 * bf_hi(w2t[0][k]) + h1 * bf_hi(w2t[1][k])};
  }
  STAMP(3);
  if (j == 0) {
#pragma nounroll
    for (unsigned spin = 0; spin < LSPIN; ++spin) {
      const u32x4 f = *(volatile u32x4*)&L.sflag[0];
      if (rfl(f.x & f.y & f.z & f.w)) break;
      if (!rfl(f.x | f.y | f.z | f.w)) __builtin_amdgcn_s_sleep(2);
    }
    CFENCE();
    STAMP(4);
    {
      const int hf = lane & 1;
      f32x4 pv[NSTR]; f32x2 pm[NSTR];
#pragma unroll
      for (int q2 = 0; q2 < NSTR; ++q2) { pv[q2] = *(const f32x4*)&L.mrg[q2][lane * 4]; pm[q2] = *(const f32x2*)&L.ml[q2][0]; }
      float MM = pm[0].x;
#pragma unroll
      for (int q2 = 1; q2 < NSTR; ++q2) MM = fmaxf(MM, pm[q2].x);
      float o0 = 0.f, o1 = 0.f, o2 = 0.f, o3 = 0.f;
#pragma unroll
      for (int q2 = 0; q2 < NSTR; ++q2) {
        const float ww = __builtin_amdgcn_exp2f(pm[q2].x - MM);
        o0 = fmaf(ww, pv[q2].x, o0); o1 = fmaf(ww, pv[q2].y, o1);
        o2 = fmaf(ww, pv[q2].z, o2); o3 = fmaf(ww, hf ? pm[q2].y : pv[q2].w, o3);
      }
      if (hf) o2 = MM;
      st_sc1(wsr, WS_P1 + b * 1024 + lane * 16, u32x4{san(o0), san(o1), san(o2), san(o3)});
    }
    STAMP(7);
    const unsigned o = WS_P1 + (h * 8 + c) * 1024 + (rq * 8 + rg) * 32;
    u32x4 A = {0u, 0u, 0u, 0u}, B = {0u, 0u, 0u, 0u};
    bool ok = false;
#pragma nounroll
    for (unsigned spin = 0; spin < GSPIN; ++spin) {
      CFENCE();
      const u32x4 nA = ld_sc1(wsr, ok ? OOB : o), nB = ld_sc1(wsr, ok ? OOB : o + 16);
      if (!ok) { A = nA; B = nB; ok = valid4(nA) && valid4(nB); }
      if (__all(ok)) break;
    }
    STAMP(5);
    const float mp = __uint_as_float(B.z);
    const float M = max_lo3(mp);
    const float w = __builtin_amdgcn_exp2f(mp - M);
    const float sc = gate * __builtin_amdgcn_rcpf(sum_lo3(w * __uint_as_float(B.w)));
    const float o0 = sum_lo3(w * __uint_as_float(A.x)) * sc, o1 = sum_lo3(w * __uint_as_float(A.y)) * sc, o2 = sum_lo3(w * __uint_as_float(A.z)) * sc;
    const float o3 = sum_lo3(w * __uint_as_float(A.w)) * sc, o4 = sum_lo3(w * __uint_as_float(B.x)) * sc, o5 = sum_lo3(w * __uint_as_float(B.y)) * sc;
    if (c == 0) { *(f32x4*)&L.os[rg * 8] = f32x4{o0, o1, o2, o3}; *(f32x2*)&L.os[rg * 8 + 4] = f32x2{o4, o5}; }
    CFENCE();
    if (lane == 0) lds_st(&L.oflag, 1u);
    CFENCE();
  } else {
    lds_wait_ge<4>(&L.sflag[0], 1u);
    lds_wait_ge<0>(&L.oflag, 1u);
  }
  {
    const f32x4 oa = *(const f32x4*)&L.os[rg * 8];
    const f32x2 ob = *(const f32x2*)&L.os[rg * 8 + 4];
#pragma unroll
    for (int t = 0; t < 6; ++t) {
      const float ov = t < 4 ? oa[t] : ob[t - 4];
#pragma unroll
      for (int k = 0; k < 4; ++k) y2[k] = __builtin_elementwise_fma(f32x2{ov, ov}, wf[t][k], y2[k]);
    }
    float r4[4];
#pragma unroll
    for (int k = 0; k < 4; ++k) { float t = tr32(y2[k >> 1][k & 1], y2[2 + (k >> 1)][k & 1]); t = tr16(t, t); r4[k] = t + dpp<XOR8>(t); }
    st_sc1(wsr, wr16 ? WS_P2 + (half * 80 + j * 16 + (col4 >> 2)) * 2048 + grp * 16 : OOB, u32x4{san(r4[0]), san(r4[1]), san(r4[2]), san(r4[3])});
  }
  STAMP(6);
  if (j == 1 && b < 160) {
    const unsigned o0 = WS_P2 + b * 2048 + lane * 16;
    u32x4 a0 = {0u, 0u, 0u, 0u}, a1 = {0u, 0u, 0u, 0u};
    bool k0 = false, k1 = false;
#pragma nounroll
    for (unsigned spin = 0; spin < GSPIN; ++spin) {
      CFENCE();
      const u32x4 n0 = ld_sc1(wsr, k0 ? OOB : o0), n1 = ld_sc1(wsr, k1 ? OOB : o0 + 1024);
      if (!k0) { a0 = n0; k0 = valid4(n0); }
      if (!k1) { a1 = n1; k1 = valid4(n1); }
      if (__all(k0 && k1)) break;
    }
    const float v0 = __uint_as_float(a0.x) + __uint_as_float(a1.x), v1 = __uint_as_float(a0.y) + __uint_as_float(a1.y);
    const float v2 = __uint_as_float(a0.z) + __uint_as_float(a1.z), v3 = __uint_as_float(a0.w) + __uint_as_float(a1.w);
    float t = tr16(tr32(v0, v2), tr32(v1, v3));
    t += dpp<XOR8>(t); t = sum_lo3(t);
    const __bf16 ov = (__bf16)t;
    __builtin_amdgcn_raw_buffer_store_b16(__builtin_bit_cast(unsigned short, ov), mkrsrc(out, D * 2), (lane & 15) == 0 ? (b * 4 + (lane >> 4)) * 2 : OOB, 0, 0);
    STAMP(7);
  }
  STAMP_FLUSH();
}

extern "C" void kernel_launch(void* const* d_in, const int* in_sizes, int n_in, void* d_out, int out_size, void* d_ws, size_t ws_size,
                              hipStream_t stream) {
  fused_kernel<<<256, NTHR, 0, stream>>>((const uint16_t*)d_in[0], (const uint16_t*)d_in[1], (const uint16_t*)d_in[2], (const uint16_t*)d_in[3],
                                         (const uint16_t*)d_in[4], (const uint16_t*)d_in[6], (const uint16_t*)d_in[7], (const uint16_t*)d_in[8],
                                         (const uint16_t*)d_in[9], (unsigned char*)d_ws, (uint16_t*)d_out);
}
```
